```python
import torch

def _softmax(x, axis=-1):
    return torch.softmax(x, dim=axis)
def _gelu(x):
    return torch.nn.functional.gelu(x, approximate="tanh")
def _silu(x):
    return torch.nn.functional.silu(x)
def _sigmoid(x):
    return torch.sigmoid(x)
def _layernorm(x, axis=-1, eps=1e-5):
    m = x.mean(dim=axis, keepdim=True)
    v = x.var(dim=axis, keepdim=True, unbiased=False)
    return (x - m) / torch.sqrt(v + eps)
def _scatter_add(tgt, idx, src, axis=0):
    return tgt.clone().index_add_(axis, idx, src)
def _segment_sum(src, seg, n):
    out = torch.zeros((n,) + src.shape[1:], dtype=src.dtype)
    return out.index_add_(0, seg, src)
def _pad(x, pw):
    flat = tuple(v for pair in reversed(pw) for v in pair)
    return torch.nn.functional.pad(x, flat)

kernel_name = 'parallel_sum__spectral_gfnet__dense_activation_gelu__prompt'

def setup_inputs():

    _w = (lambda a: (lambda t: t.to(torch.bfloat16) if t.is_floating_point() else t)(torch.as_tensor(a)))
    def _A_setup():
        B, S, D = 1, 2048, 448
        Df = D // 2 + 1
        return {
            "x": _w((lambda *s: torch.randn(*s, dtype=torch.bfloat16) * 0.05)(B, S, D)),
            "w_r": _w((lambda *s: torch.randn(*s, dtype=torch.bfloat16) * 0.8)(S, Df) * 0.02),
            "w_i": _w((lambda *s: torch.randn(*s, dtype=torch.bfloat16) * 0.8)(S, Df) * 0.02),
        }
    _A_in = _A_setup()

    def _B_setup():
        B, S, D, F = 1, 2048, 448, 1792
        return {
            "x": _w((lambda *s: torch.randn(*s, dtype=torch.bfloat16) * 0.05)(B, S, D)),
            "w1": _w((lambda *s: torch.randn(*s, dtype=torch.bfloat16) * 0.4)(D, F) * (D ** -0.5)),
            "b1": _w((lambda *s: torch.randn(*s, dtype=torch.bfloat16) * 12.8)(F) * 0.01),
            "w2": _w((lambda *s: torch.randn(*s, dtype=torch.bfloat16) * 0.2)(F, D) * (F ** -0.5)),
            "b2": _w((lambda *s: torch.randn(*s, dtype=torch.bfloat16) * 1.6)(D) * 0.01),
        }
    _B_in = _B_setup()

    return {
        "A_x": _A_in["x"],
        "A_w_r": _A_in["w_r"],
        "A_w_i": _A_in["w_i"],
        "B_x": _B_in["x"],
        "B_w1": _B_in["w1"],
        "B_b1": _B_in["b1"],
        "B_w2": _B_in["w2"],
        "B_b2": _B_in["b2"],
    }

def reference(A_x, A_w_r, A_w_i, B_x, B_w1, B_b1, B_w2, B_b2):
    def _A_ref(x, w_r, w_i):
        B, S, D = x.shape
        X = (lambda x, axes=(-2,-1): torch.fft.rfft2(x.float(), dim=axes))(x, axes=(-2, -1))
        W = w_r + 1j * w_i
        X = X * W
        y = (lambda x, s, axes=(-2,-1): torch.fft.irfft2(x, s=s, dim=axes).to(torch.bfloat16))(X, (S, D), axes=(-2, -1))
        return y

    def _B_ref(x, w1, b1, w2, b2):
        h = torch.einsum("bsd,df->bsf", x, w1) + b1
        h = _gelu(h)
        return torch.einsum("bsf,fd->bsd", h, w2) + b2

    x = A_x
    xn = _layernorm(x, axis=-1)
    return x + _A_ref(xn, A_w_r, A_w_i) + _B_ref(xn, B_w1, B_b1, B_w2, B_b2)
```

```cpp
#include <hip/hip_runtime.h>
#include <stdint.h>
#pragma clang diagnostic ignored "-Wint-to-pointer-cast"

#define DEV __device__ __forceinline__
#define LAS __attribute__((address_space(3)))
typedef __attribute__((ext_vector_type(4))) short s16x4;
typedef __attribute__((ext_vector_type(8))) short s16x8;
typedef __attribute__((ext_vector_type(8))) __bf16 bf16x8;
typedef __attribute__((ext_vector_type(2))) __bf16 bf16x2;
typedef __attribute__((ext_vector_type(8))) _Float16 f16x8;
typedef __attribute__((ext_vector_type(2))) _Float16 f16x2;
typedef __attribute__((ext_vector_type(2))) float f32x2;
typedef __attribute__((ext_vector_type(4))) float f32x4;
typedef __attribute__((ext_vector_type(16))) float f32x16;
typedef __attribute__((ext_vector_type(2))) unsigned u32x2;
typedef __attribute__((ext_vector_type(4))) unsigned u32x4;
typedef __amdgpu_buffer_rsrc_t rsrc_t;

constexpr unsigned WS_E = 0;
constexpr unsigned WS_ZF = 4096;
constexpr unsigned WS_HF = WS_ZF + 224 * 128;
constexpr unsigned WS_AF = WS_HF + 224 * 128;
constexpr unsigned WS_Z = 65536;
constexpr unsigned WS_A = WS_Z + 29 * 16 * 4096;
constexpr unsigned WS_H = WS_A + 32 * 2 * 29 * 1024;
constexpr unsigned WS_END = WS_H + 32 * 112 * 2048;
constexpr unsigned OOB = 0x80000000u;
constexpr int SPIN_CAP = 1 << 16;

constexpr unsigned L_MISC = 147456;
constexpr unsigned L_STAT = L_MISC;
constexpr unsigned L_RROW = L_STAT + 4096;
constexpr unsigned L_CS = L_RROW + 1024;
constexpr unsigned L_GATH = 4 * 16384;
constexpr unsigned L_W2R1 = 4 * 16384;
DEV constexpr unsigned xblk(int kc) { return (unsigned)((kc + 8) % 9) * 16384u; }
DEV constexpr unsigned w1blk(int bi) { return (unsigned)((bi + 6) % 9) * 16384u; }
DEV constexpr unsigned w1row(int n) { return w1blk(n >> 7) + (unsigned)(n & 127) * 128u; }

#define WAITBAR(n) asm volatile("s_waitcnt vmcnt(" #n ") lgkmcnt(0)\n\ts_barrier" ::: "memory")
#define LBAR() asm volatile("s_waitcnt lgkmcnt(0)\n\ts_barrier" ::: "memory")
#define PIN(x) asm volatile("" : "+v"(x))
#define SB() __builtin_amdgcn_sched_barrier(0)
#define STAMP(i)
#define STAMP_OUT()
#define WAITVM(n) asm volatile("s_waitcnt vmcnt(" #n ")" ::: "memory")

DEV u32x4 mk_srd(const void* p, unsigned bytes) {
  uint64_t a = (uint64_t)p;
  u32x4 r = {(unsigned)a, (unsigned)(a >> 32) & 0xffffu, bytes, 0x00020000u};
  return r;
}
DEV void dma16(unsigned lds, unsigned voff, u32x4 srd, unsigned soff) {
  asm volatile("s_mov_b32 m0, %0\n\ts_nop 0\n\tbuffer_load_dwordx4 %1, %2, %3 offen lds"
               :: "s"(lds), "v"(voff), "s"(srd), "s"(soff) : "memory");
}
DEV u32x4 lds128(unsigned a) { return *(LAS u32x4*)a; }
DEV void lds128w(unsigned a, u32x4 v) { *(LAS u32x4*)a = v; }
DEV u32x4 trfrag(unsigned a0, unsigned a1) {
  s16x4 lo = __builtin_amdgcn_ds_read_tr16_b64_v4i16((LAS s16x4*)a0);
  s16x4 hi = __builtin_amdgcn_ds_read_tr16_b64_v4i16((LAS s16x4*)a1);
  return __builtin_bit_cast(u32x4, __builtin_shufflevector(lo, hi, 0, 1, 2, 3, 4, 5, 6, 7));
}
DEV f32x16 mfma_bf(u32x4 a, u32x4 b, f32x16 c) {
  return __builtin_amdgcn_mfma_f32_32x32x16_bf16(__builtin_bit_cast(bf16x8, a), __builtin_bit_cast(bf16x8, b), c, 0, 0, 0);
}
DEV f32x16 mfma_h(u32x4 a, u32x4 b, f32x16 c) {
  return __builtin_amdgcn_mfma_f32_32x32x16_f16(__builtin_bit_cast(f16x8, a), __builtin_bit_cast(f16x8, b), c, 0, 0, 0);
}
DEV unsigned pk_bf(float lo, float hi) {
  f32x2 p = {lo, hi};
  return __builtin_bit_cast(unsigned, __builtin_convertvector(p, bf16x2));
}
DEV unsigned pk_h(float lo, float hi) {
  f32x2 p = {lo, hi};
  return __builtin_bit_cast(unsigned, __builtin_convertvector(p, f16x2));
}
DEV unsigned pk_bf2(f32x2 p) { return __builtin_bit_cast(unsigned, __builtin_convertvector(p, bf16x2)); }
DEV unsigned pk_h2(f32x2 p) { return __builtin_bit_cast(unsigned, __builtin_convertvector(p, f16x2)); }
DEV f32x2 pr(const f32x16& v, int r) { return f32x2{v[r], v[r + 1]}; }
DEV f32x2 fma2(f32x2 a, f32x2 b, f32x2 c) { return __builtin_elementwise_fma(a, b, c); }
DEV f32x2 bc(float a) { return f32x2{a, a}; }
DEV float bf_lo(unsigned v) { return __builtin_bit_cast(float, v << 16); }
DEV float bf_hi(unsigned v) { return __builtin_bit_cast(float, v & 0xffff0000u); }
DEV float bf16r(float x) { return bf_lo(pk_bf(x, 0.f)); }
DEV float dot2(unsigned a, unsigned b, float c) {
  return __builtin_amdgcn_fdot2_f32_bf16(__builtin_bit_cast(bf16x2, a), __builtin_bit_cast(bf16x2, b), c, false);
}
DEV float cosr(float rev) { return __builtin_amdgcn_cosf(rev); }
DEV float sinr(float rev) { return __builtin_amdgcn_sinf(rev); }
DEV float rev448(float n) {
  float q = __builtin_floorf(n * (1.0f / 448.0f));
  return __builtin_fmaf(q, -448.0f, n) * (1.0f / 448.0f);
}
DEV void poll(u32x4 srd, unsigned voff, u32x2 tag) {
#pragma unroll 1
  for (int n = 0; n < SPIN_CAP; ++n) {
    u32x2 v;
    asm volatile("buffer_load_dwordx2 %0, %1, %2, 0 offen sc1\n\ts_waitcnt vmcnt(0)"
                 : "=&v"(v) : "v"(voff), "s"(srd) : "memory");
    if (__builtin_amdgcn_ballot_w64(v[0] != tag[0] || v[1] != tag[1]) == 0) break;
    __builtin_amdgcn_s_sleep(1);
  }
}
DEV unsigned sel_x1(bool keep, unsigned own, unsigned other) {
  unsigned o = __builtin_amdgcn_update_dpp(0u, other, 0xB1, 0xf, 0xf, true);
  return keep ? own : o;
}
DEV unsigned sel_x2(bool keep, unsigned own, unsigned other) {
  unsigned o = __builtin_amdgcn_update_dpp(0u, other, 0x4E, 0xf, 0xf, true);
  return keep ? own : o;
}

__global__ void __launch_bounds__(512, 2)
fused_kernel(const void* __restrict__ px, const void* __restrict__ pwr, const void* __restrict__ pwi,
             const void* __restrict__ pw1, const void* __restrict__ pb1, const void* __restrict__ pw2,
             const void* __restrict__ pb2, void* __restrict__ pout, void* __restrict__ pws) {
  __shared__ __attribute__((aligned(1024))) char smem[163840];
  const unsigned LB = (unsigned)(uintptr_t)(LAS char*)smem;
  const int tid = threadIdx.x;
  const int lane = tid & 63;
  const int w = __builtin_amdgcn_readfirstlane(tid >> 6);
  const int l31 = lane & 31, h = lane >> 5;
  const int xcd = blockIdx.x & 7, loc = blockIdx.x >> 3;

  const u32x4 sws = mk_srd(pws, WS_END);
  const rsrc_t rws = __builtin_amdgcn_make_buffer_rsrc(pws, 0, WS_END, 0x00020000);

  STAMP(0);
  u32x2 ev;
  asm volatile("s_nop 4\n\tbuffer_load_dwordx2 %0, off, %1, 0 sc1" : "=v"(ev) : "s"(sws) : "memory");

  if (loc >= 28) {
    const int pq = xcd, qt = loc - 28;
    const bool active = (w < 7) || (qt == 3);
    const int g = (w < 7) ? 7 * qt + w : 28;
    const int kdl = lane & 7, pl = (lane >> 3) & 3;
    const int p = 4 * pq + pl, kd = 8 * g + kdl;
    WAITVM(0);
    asm volatile("" : "+v"(ev));
    uint64_t e64 = ((uint64_t)__builtin_amdgcn_readfirstlane(ev[1]) << 32) | (unsigned)__builtin_amdgcn_readfirstlane(ev[0]);
    e64 = e64 * 6364136223846793005ull + 1442695040888963407ull;
    const u32x2 tag = {(unsigned)e64, (unsigned)(e64 >> 32)};
    if (active) {
      const rsrc_t rwr = __builtin_amdgcn_make_buffer_rsrc((void*)pwr, 0, 2048 * 225 * 2, 0x00020000);
      const rsrc_t rwi = __builtin_amdgcn_make_buffer_rsrc((void*)pwi, 0, 2048 * 225 * 2, 0x00020000);
      f32x2 wpr[8], wpi[8], wmr[8], wmi[8];
      const float wsc = ((kd == 0 || kd == 224) ? 1.0f : 2.0f) / (2048.0f * 448.0f);
#pragma unroll
      for (int r = 0; r < 16; ++r) {
        const int k2 = (r & 3) + 8 * (r >> 2) + 4 * h;
        const int kp = p + 64 * k2;
        const int km = p ? ((2048 - p + 64 * k2) & 2047) : (32 + 64 * k2);
        const unsigned op = kd > 224 ? OOB : (unsigned)(kp * 225 + kd) * 2u;
        const unsigned om = kd > 224 ? OOB : (unsigned)(km * 225 + kd) * 2u;
        wpr[r >> 1][r & 1] = bf_lo(__builtin_amdgcn_raw_buffer_load_b16(rwr, op, 0, 0));
        wpi[r >> 1][r & 1] = bf_lo(__builtin_amdgcn_raw_buffer_load_b16(rwi, op, 0, 0));
        wmr[r >> 1][r & 1] = bf_lo(__builtin_amdgcn_raw_buffer_load_b16(rwr, om, 0, 0));
        wmi[r >> 1][r & 1] = bf_lo(__builtin_amdgcn_raw_buffer_load_b16(rwi, om, 0, 0));
      }
      u32x4 FR[4], FI[4], IR[4], II[4];
#pragma unroll
      for (int t = 0; t < 4; ++t) {
        float c[4], sn[4];
#pragma unroll
        for (int e = 0; e < 4; ++e) {
          const float a = (float)(l31 * (8 * t + 4 * h + e)) * (1.0f / 32.0f);
          c[e] = cosr(a);
          sn[e] = sinr(a);
          FR[t][e] = pk_h(c[e], sn[e]);
          FI[t][e] = pk_h(-sn[e], c[e]);
        }
        IR[t] = u32x4{pk_bf(c[0], c[1]), pk_bf(c[2], c[3]), pk_bf(-sn[0], -sn[1]), pk_bf(-sn[2], -sn[3])};
        II[t] = u32x4{pk_bf(sn[0], sn[1]), pk_bf(sn[2], sn[3]), pk_bf(c[0], c[1]), pk_bf(c[2], c[3])};
        PIN(FR[t]); PIN(FI[t]); PIN(IR[t]); PIN(II[t]);
      }
      float ff = pq ? 1.0f : 0.0f;
      float cph = pq ? 1.0f : cosr((float)l31 * (1.0f / 64.0f));
      float sph = pq ? 0.0f : sinr((float)l31 * (1.0f / 64.0f));
      PIN(ff); PIN(cph); PIN(sph);
#pragma unroll
      for (int r = 0; r < 8; ++r) {
        wpr[r] *= bc(wsc); wpi[r] *= bc(wsc); wmr[r] *= bc(wsc); wmi[r] *= bc(wsc);
        PIN(wpr[r]); PIN(wpi[r]); PIN(wmr[r]); PIN(wmi[r]);
      }
      unsigned zo = (kd > 224) ? OOB : WS_Z + (unsigned)((g * 16 + 2 * h) * 4096 + p * 128 + kdl * 16);
      unsigned ao = WS_A + (unsigned)((((lane & 28) * 2 + (pq >> 2)) * 29 + g) * 1024 + (32 * h + 8 * (pq & 3) + (lane & 3)) * 16);
      PIN(zo); PIN(ao);
      STAMP(1);
      const int jz = (g == 28) ? 0 : (g >> 1);
      poll(sws, WS_ZF + (unsigned)((lane & 15) * 14 + jz) * 128u, tag);
      STAMP(2);

      u32x4 z[8];
#pragma unroll
      for (int i = 0; i < 8; ++i)
        z[i] = __builtin_amdgcn_raw_buffer_load_b128(rws, zo, (4 * (i >> 1) + (i & 1)) * 4096, 16);
      SB();
      f32x16 xpr = {}, xpi = {}, xmr = {}, xmi = {};
#pragma unroll
      for (int t = 0; t < 4; ++t) {
        const u32x4 zp = {z[2 * t][0], z[2 * t][1], z[2 * t + 1][0], z[2 * t + 1][1]};
        const u32x4 zm = {z[2 * t][2], z[2 * t][3], z[2 * t + 1][2], z[2 * t + 1][3]};
        xpr = mfma_h(FR[t], zp, xpr);
        xpi = mfma_h(FI[t], zp, xpi);
        xmr = mfma_h(FR[t], zm, xmr);
        xmi = mfma_h(FI[t], zm, xmi);
      }
      STAMP(3);
      f32x16 ypr = {}, ypi = {}, ymr = {}, ymi = {};
#pragma unroll
      for (int t = 0; t < 4; ++t) {
        u32x4 pp, pm;
#pragma unroll
        for (int e = 0; e < 2; ++e) {
          const int r = 4 * t + 2 * e, q = 2 * t + e;
          pp[e] = pk_bf2(fma2(pr(xpr, r), wpr[q], -(pr(xpi, r) * wpi[q])));
          pp[2 + e] = pk_bf2(fma2(pr(xpr, r), wpi[q], pr(xpi, r) * wpr[q]));
          pm[e] = pk_bf2(fma2(pr(xmr, r), wmr[q], -(pr(xmi, r) * wmi[q])));
          pm[2 + e] = pk_bf2(fma2(pr(xmr, r), wmi[q], pr(xmi, r) * wmr[q]));
        }
        ypr = mfma_bf(pp, IR[t], ypr);
        ypi = mfma_bf(pp, II[t], ypi);
        ymr = mfma_bf(pm, IR[t], ymr);
        ymi = mfma_bf(pm, II[t], ymi);
      }
      STAMP(4);
      u32x4 un[8];
#pragma unroll
      for (int r = 0; r < 16; r += 2) {
        f32x2 acr, aci, asr, asi;
        if (r < 4) {
          acr = fma2(bc(ff), pr(ymr, r), pr(ypr, r));
          aci = fma2(bc(ff), pr(ymi, r), pr(ypi, r));
          asr = fma2(bc(cph), pr(ymi, r), fma2(bc(sph), pr(ymr, r), -(bc(ff) * pr(ypi, r))));
          asi = fma2(bc(sph), pr(ymi, r), fma2(bc(ff), pr(ypr, r), -(bc(cph) * pr(ymr, r))));
        } else {
          acr = pr(ypr, r) + pr(ymr, r);
          aci = pr(ypi, r) + pr(ymi, r);
          asr = pr(ymi, r) - pr(ypi, r);
          asi = pr(ypr, r) - pr(ymr, r);
        }
        un[2 * (r >> 2)][(r >> 1) & 1] = pk_bf2(acr);
        un[2 * (r >> 2)][2 + ((r >> 1) & 1)] = pk_bf2(aci);
        un[2 * (r >> 2) + 1][(r >> 1) & 1] = pk_bf2(asr);
        un[2 * (r >> 2) + 1][2 + ((r >> 1) & 1)] = pk_bf2(asi);
      }
      const bool b0 = lane & 1, b1 = lane & 2;
#pragma unroll
      for (int st = 0; st < 2; ++st) {
        u32x4 a[4], b[4];
#pragma unroll
        for (int e = 0; e < 4; ++e) {
          a[0][e] = sel_x1(!b0, un[4 * st + 0][e], un[4 * st + 1][e]);
          a[1][e] = sel_x1(b0, un[4 * st + 1][e], un[4 * st + 0][e]);
          a[2][e] = sel_x1(!b0, un[4 * st + 2][e], un[4 * st + 3][e]);
          a[3][e] = sel_x1(b0, un[4 * st + 3][e], un[4 * st + 2][e]);
          b[0][e] = sel_x2(!b1, a[0][e], a[2][e]);
          b[2][e] = sel_x2(b1, a[2][e], a[0][e]);
          b[1][e] = sel_x2(!b1, a[1][e], a[3][e]);
          b[3][e] = sel_x2(b1, a[3][e], a[1][e]);
        }
#pragma unroll
        for (int t = 0; t < 4; ++t)
          __builtin_amdgcn_raw_buffer_store_b128(b[t], rws, ao + 64 * st + t * 2 * 29 * 1024, 0, 16);
      }
    }
    STAMP(5);
    WAITBAR(0);
    if (tid == 0) __builtin_amdgcn_raw_buffer_store_b64(tag, rws, WS_AF + (unsigned)(pq * 4 + qt) * 128u, 0, 16);
    STAMP(6);
    STAMP_OUT();
    return;
  }

  const int rg = 4 * (xcd >> 1) + loc / 7, j = 7 * (xcd & 1) + loc % 7;
  const int s2 = 4 * xcd + loc / 7, cg = loc % 7;
  const int rgw = w & 3, cgw = w >> 2;
  const u32x4 sx = mk_srd(px, 2048 * 448 * 2), sw1 = mk_srd(pw1, 448 * 1792 * 2), sw2 = mk_srd(pw2, 448 * 1792 * 2);
  const u32x4 sb1 = mk_srd(pb1, 1792 * 2);
  unsigned b1v[2];
  {
    const unsigned o = h ? OOB : (unsigned)(128 * j + 32 * cgw + l31) * 2u;
    asm volatile("buffer_load_ushort %0, %2, %3, 0 offen\n\tbuffer_load_ushort %1, %2, %3, 0 offen offset:128"
                 : "=&v"(b1v[0]), "=&v"(b1v[1]) : "v"(o), "s"(sb1) : "memory");
  }
  {
    const int rho = 8 * w + (lane >> 3);
    const unsigned vx = (unsigned)((32 * rho + 2 * rg) * 896 + (((lane & 7) ^ ((rho >> 1) & 7)) * 16));
#pragma unroll
    for (int kc = 0; kc < 7; ++kc) {
      dma16(LB + xblk(kc) + w * 1024, vx, sx, kc * 128);
      dma16(LB + xblk(kc) + 8192 + w * 1024, vx, sx, kc * 128 + 896);
    }
  }
  const unsigned swz16 = (unsigned)(((lane & 7) ^ ((lane >> 2) & 4)) * 16);
  const unsigned vw1 = (unsigned)(lane >> 3) * 3584u + swz16;
  auto w1dma = [&](int bi) {
#pragma unroll
    for (int q = 0; q < 2; ++q) {
      const int n0 = 128 * bi + 64 * q + 8 * w;
      const int so = (n0 >= 448) ? (n0 - 448) * 3584 + 128 : n0 * 3584;
      dma16(LB + w1blk(bi) + q * 8192 + w * 1024, vw1, sw1, so + 256 * j);
    }
  };
  w1dma(0);
  w1dma(1);
  STAMP(1);

  u32x4 F[14];
  u32x4 faug;
  {
    const int c = l31 & 15, uv = l31 >> 4;
    const bool sp = (j == 0) && (l31 == 16);
    const float kdf = sp ? 224.0f : (float)(16 * j + c);
    const float ph = (uv && !sp) ? 0.25f : 0.0f;
    const float tw = 2.0f * cosr(rev448(32.0f * kdf));
    f32x2 f0[4], f1[4];
#pragma unroll
    for (int e = 0; e < 8; ++e) {
      const float d = (float)(16 * cgw + 8 * h + e);
      f0[e >> 1][e & 1] = cosr(rev448(d * kdf) + ph);
      f1[e >> 1][e & 1] = cosr(rev448((d + 32.0f) * kdf) + ph);
    }
    float cs = 0.f;
#pragma unroll
    for (int i = 0; i < 14; ++i) {
#pragma unroll
      for (int e = 0; e < 4; ++e) {
        F[i][e] = pk_bf2(f0[e]);
        cs = dot2(F[i][e], 0x3f803f80u, cs);
        const f32x2 n = fma2(bc(tw), f1[e], -f0[e]);
        f0[e] = f1[e];
        f1[e] = n;
      }
      PIN(F[i]);
    }
    const float hi = bf16r(cs);
    faug = u32x4{pk_bf(hi, cs - hi), 0u, 0u, 0u};
    PIN(faug);
  }
  u32x4 GA[2][2];
  {
    const int hh = (l31 >> 2) & 1, reg = (l31 & 3) + 4 * (l31 >> 3);
    const int s2a = 2 * rg + (rgw >> 1);
#pragma unroll
    for (int mt = 0; mt < 2; ++mt) {
      const int p = 16 * mt + 8 * hh + (reg >> 1), cs = reg & 1;
      const bool sp = (p == 0) && cs;
      const float ca = sp ? 0.5f : (float)p * (1.0f / 64.0f);
      const float cb = sp ? 0.0f : (float)(p * s2a) * (1.0f / 2048.0f) - 0.25f * (float)cs;
#pragma unroll
      for (int t = 0; t < 2; ++t)
#pragma unroll
        for (int e = 0; e < 4; ++e) {
          const float s1 = (float)(32 * (rgw & 1) + 16 * t + 8 * (e >> 1) + 4 * h + 2 * (e & 1));
          GA[mt][t][e] = pk_h(cosr(__builtin_fmaf(ca, s1, cb)), cosr(__builtin_fmaf(ca, s1 + 1.0f, cb)));
        }
      PIN(GA[mt][0]); PIN(GA[mt][1]);
    }
  }

  STAMP(2);
  u32x4 X[28];
  f32x16 U = {};
  float sum = 0.f, sq = 0.f;
  {
    const unsigned xr = LB + (unsigned)(32 * rgw + l31) * 128u;
    const unsigned xs = (unsigned)((2 * cgw + h) ^ ((l31 >> 1) & 7));
#define XSTEP(kc, n)                                                          \
    WAITBAR(n);                                                               \
    if (kc >= 1 && kc <= 5) w1dma(kc + 1);                                    \
    _Pragma("unroll") for (int q = 0; q < 4; ++q)                             \
      X[4 * kc + q] = lds128(xr + ((xs ^ (2 * q)) * 16) + xblk(kc));          \
    _Pragma("unroll") for (int q = 0; q < 4; q += 2) {                        \
      U = mfma_bf(X[4 * kc + q], F[2 * kc + (q >> 1)], U);                    \
      _Pragma("unroll") for (int e = 0; e < 4; ++e) {                         \
        sum = dot2(X[4 * kc + q][e], 0x3f803f80u, sum);                       \
        sq = dot2(X[4 * kc + q][e], X[4 * kc + q][e], sq);                    \
      }                                                                       \
    }
    XSTEP(0, 16) STAMP(3); XSTEP(1, 14) XSTEP(2, 14) XSTEP(3, 14) XSTEP(4, 14) XSTEP(5, 14) XSTEP(6, 14)
#undef XSTEP
  }
  asm volatile("" : "+v"(ev), "+v"(b1v[0]), "+v"(b1v[1]));
  uint64_t e64 = ((uint64_t)__builtin_amdgcn_readfirstlane(ev[1]) << 32) | (unsigned)__builtin_amdgcn_readfirstlane(ev[0]);
  e64 = e64 * 6364136223846793005ull + 1442695040888963407ull;
  const u32x2 tag = {(unsigned)e64, (unsigned)(e64 >> 32)};

  *(LAS f32x2*)(LB + L_STAT + (unsigned)((32 * rgw + l31) * 4 + 2 * cgw + h) * 8u) = f32x2{sum, sq};
  STAMP(4);
  LBAR();
  float sig, rstd;
  u32x4 xaug;
  {
    const f32x4 a = *(LAS f32x4*)(LB + L_STAT + (unsigned)(32 * rgw + l31) * 32u);
    const f32x4 b = *(LAS f32x4*)(LB + L_STAT + (unsigned)(32 * rgw + l31) * 32u + 16u);
    const float mean = ((a[0] + a[2]) + (b[0] + b[2])) * (1.0f / 448.0f);
    const float ex2 = ((a[1] + a[3]) + (b[1] + b[3])) * (1.0f / 448.0f);
    const float var = __builtin_fmaxf(__builtin_fmaf(-mean, mean, ex2), 0.0f);
    sig = bf16r(__builtin_amdgcn_sqrtf(bf16r(bf16r(var) + 1e-5f)));
    rstd = 1.0f / sig;
    xaug = u32x4{pk_bf(-mean, -mean), pk_bf(sig, 0.f), 0u, 0u};
  }
  *(LAS float*)(LB + L_RROW + (unsigned)(w * 32 + l31) * 4u) = rstd;
  U = mfma_bf(xaug, faug, U);
  {
    u32x4 ub[2];
#pragma unroll
    for (int q = 0; q < 4; ++q) {
      const f32x4 rr = *(LAS f32x4*)(LB + L_RROW + (unsigned)(w * 32 + 8 * q + 4 * h) * 4u);
      ub[q >> 1][2 * (q & 1)] = pk_h2(pr(U, 4 * q) * f32x2{rr[0], rr[1]});
      ub[q >> 1][2 * (q & 1) + 1] = pk_h2(pr(U, 4 * q + 2) * f32x2{rr[2], rr[3]});
    }
#pragma unroll
    for (int mt = 0; mt < 2; ++mt) {
      f32x16 za = {};
      za = mfma_h(GA[mt][0], ub[0], za);
      za = mfma_h(GA[mt][1], ub[1], za);
#pragma unroll
      for (int q = 0; q < 2; ++q) {
        u32x4 v;
#pragma unroll
        for (int e = 0; e < 4; ++e) v[e] = pk_h(za[8 * q + 2 * e], za[8 * q + 2 * e + 1]);
        lds128w(LB + L_GATH + (unsigned)(w * 4096 + l31 * 128 + (((4 * mt + 2 * h + q) ^ (l31 & 7)) * 16)), v);
      }
    }
  }
  STAMP(5);
  WAITBAR(10);
  STAMP(6);
  {
    const int gl = tid >> 8, p = (tid >> 3) & 31, kdl = tid & 7;
    const unsigned ga = LB + L_GATH + (unsigned)((8 * gl + kdl) * 128 + (((p >> 2) ^ kdl) * 16) + (p & 3) * 4);
    const bool sp = (j == 0) && (gl == 0) && (kdl == 0);
    const float ff = p ? 1.0f : 0.0f;
    u32x4 z, zx;
#pragma unroll
    for (int sg = 0; sg < 2; ++sg) {
      float v[4];
#pragma unroll
      for (int uv = 0; uv < 2; ++uv) {
        f16x2 t[4];
#pragma unroll
        for (int k = 0; k < 4; ++k)
          t[k] = *(LAS f16x2*)(ga + (unsigned)((2 * sg + (k & 1) + 4 * (k >> 1)) * 4096 + uv * 2048));
        const f16x2 ts = (t[0] + t[1]) + (t[2] + t[3]);
        v[2 * uv] = (float)ts[0];
        v[2 * uv + 1] = (float)ts[1];
      }
      const float ang = (float)(2 * rg + sg) * (1.0f / 64.0f);
      const float cph = p ? 1.0f : cosr(ang), sph = p ? 0.0f : sinr(ang);
      const float a = v[0], e = v[1], c = sp ? 0.f : v[2], b = sp ? 0.f : v[3];
      float o[8] = {a + ff * b, c - ff * e, ff * a - (cph * b - sph * e), ff * c + (cph * e + sph * b),
                    v[2], -ff * v[3], ff * v[2] + sph * v[3], cph * v[3]};
#pragma unroll
      for (int k = 0; k < 8; ++k) asm("" : "+v"(o[k]));
      z[sg] = pk_h(o[0], o[1]);
      z[2 + sg] = pk_h(o[2], o[3]);
      zx[sg] = pk_h(o[4], o[5]);
      zx[2 + sg] = pk_h(o[6], o[7]);
    }
    __builtin_amdgcn_raw_buffer_store_b128(z, rws, WS_Z + (unsigned)(((2 * j + gl) * 16 + rg) * 4096 + (tid & 255) * 16), 0, 16);
    __builtin_amdgcn_raw_buffer_store_b128(zx, rws, sp ? WS_Z + (unsigned)((28 * 16 + rg) * 4096 + p * 128) : OOB, 0, 16);
  }

  SB();
  STAMP(7);
  const int tq = (lane >> 2) & 3, tp = lane & 3, tg = (lane >> 4) & 1;
  const unsigned w1b = LB + (unsigned)((8 * h + tq) * 128 + 64 * (cgw ^ (tq >> 1)) + 32 * tg + 8 * tp);
  const unsigned w1e = w1b + 2048u * cgw, w1o = w1b + 2048u * (1 - cgw);
  auto w1frag = [&](int c, int u) {
    const unsigned a = ((u & 1) ? w1o : w1e) + w1row(448 * c + 16 * (u & ~1));
    return trfrag(a, a + 512);
  };
  auto colsum = [&](int c) {
    float cs = 0.f;
#pragma unroll
    for (int i = 0; i < 7; ++i) {
      const unsigned a = w1b + 2048u * rgw + w1row(448 * c + 64 * i);
      const u32x4 f = trfrag(a, a + 512);
#pragma unroll
      for (int e = 0; e < 4; ++e) cs = dot2(f[e], 0x3f803f80u, cs);
    }
    *(LAS float*)(LB + L_CS + (unsigned)(((c * 2 + cgw) * 64 + lane) * 4 + rgw) * 4u) = cs;
  };
  const float gc1 = -2.302208198f * rstd, gc2 = gc1 * 0.044715f * rstd * rstd;
  const unsigned ho = WS_H + (unsigned)((((2 * rg + (rgw >> 1)) * 112 + 8 * j + 2 * cgw) * 2 + (rgw & 1)) * 1024 + lane * 16);
  auto finish = [&](int c, f32x16 acc) {
    const f32x4 pc = *(LAS f32x4*)(LB + L_CS + (unsigned)((c * 2 + cgw) * 64 + lane) * 16u);
    const float cs = ((pc[0] + pc[1]) + pc[2]) + pc[3];
    const float hi = bf16r(cs);
    const u32x4 waug = {pk_bf(hi, cs - hi), b1v[c], 0u, 0u};
    acc = mfma_bf(waug, xaug, acc);
#pragma unroll
    for (int t = 0; t < 2; ++t) {
      u32x4 o;
#pragma unroll
      for (int e = 0; e < 4; ++e) {
        const f32x2 u = pr(acc, 8 * t + 2 * e);
        const f32x2 ar = fma2(u * u, bc(gc2), bc(gc1)) * u;
        const f32x2 ex = {__builtin_amdgcn_exp2f(ar[0]), __builtin_amdgcn_exp2f(ar[1])};
        const f32x2 dn = fma2(ex, bc(sig), bc(sig));
        o[e] = pk_bf2(u * f32x2{__builtin_amdgcn_rcpf(dn[0]), __builtin_amdgcn_rcpf(dn[1])});
      }
      __builtin_amdgcn_raw_buffer_store_b128(o, rws, ho + t * 2048 + c * 8192, 0, 16);
    }
  };
  f32x16 h0 = {}, h1 = {};
#pragma unroll
  for (int u = 0; u < 16; ++u) h0 = mfma_bf(w1frag(0, u), X[u], h0);
  STAMP(8);
  WAITBAR(8);
  colsum(0);
#pragma unroll
  for (int u = 16; u < 28; ++u) h0 = mfma_bf(w1frag(0, u), X[u], h0);
  STAMP(9);
  WAITBAR(0);
  STAMP(10);
  if (tid == 0) __builtin_amdgcn_raw_buffer_store_b64(tag, rws, WS_ZF + (unsigned)(rg * 14 + j) * 128u, 0, 16);
  const unsigned vw2 = (unsigned)(lane >> 3) * 896u + swz16;
  const unsigned sw2b = (unsigned)(224 * w * 896 + 128 * cg);
#pragma unroll
  for (int pc = 0; pc < 10; ++pc) dma16(LB + L_W2R1 + w * 10240 + pc * 1024, vw2, sw2, sw2b + pc * 7168);
  colsum(1);
  finish(0, h0);
#pragma unroll
  for (int u = 0; u < 28; ++u) h1 = mfma_bf(w1frag(1, u), X[u], h1);
  STAMP(11);
  WAITVM(2);
  const unsigned w2l = (unsigned)((4 * h + tq) * 128 + 64 * (tq >> 1) + 32 * tg + 8 * tp);
  u32x4 W2R[5][2];
#pragma unroll
  for (int t = 0; t < 5; ++t)
#pragma unroll
    for (int x = 0; x < 2; ++x) {
      const unsigned a = LB + L_W2R1 + w * 10240 + (w2l ^ (64u * x)) + t * 2048;
      W2R[t][x] = trfrag(a, a + 1024);
    }
  LBAR();
  STAMP(12);
  finish(1, h1);
  STAMP(13);
#pragma unroll
  for (int pc = 10; pc < 28; ++pc) dma16(LB + w * 18432 + (pc - 10) * 1024, vw2, sw2, sw2b + pc * 7168);
  STAMP(14);
  WAITBAR(18);
  STAMP(15);
  if (tid == 0) __builtin_amdgcn_raw_buffer_store_b64(tag, rws, WS_HF + (unsigned)(rg * 14 + j) * 128u, 0, 16);

  const rsrc_t rx = __builtin_amdgcn_make_buffer_rsrc((void*)px, 0, 2048 * 448 * 2, 0x00020000);
  const rsrc_t rb2 = __builtin_amdgcn_make_buffer_rsrc((void*)pb2, 0, 448 * 2, 0x00020000);
  const rsrc_t rout = __builtin_amdgcn_make_buffer_rsrc(pout, 0, 2048 * 448 * 2, 0x00020000);
  const unsigned oo = (unsigned)((32 * (tid >> 3) + s2) * 896 + 128 * cg + (tid & 7) * 16);
  const u32x4 xres = __builtin_amdgcn_raw_buffer_load_b128(rx, oo, 0, 0);
  const u32x4 b2v = __builtin_amdgcn_raw_buffer_load_b128(rb2, (unsigned)(128 * cg + (tid & 7) * 16), 0, 0);
  {
    const int j0 = (224 * w) >> 7, j1 = (224 * w + 223) >> 7;
    const int jj = min(j0 + (lane & 3), j1);
    poll(sws, WS_HF + (unsigned)((s2 >> 1) * 14 + jj) * 128u, tag);
  }
  STAMP(16);
  f32x16 acc[2][2] = {};
  {
    u32x4 H[14][2];
    const unsigned hi = WS_H + (unsigned)((s2 * 112 + 14 * w) * 2048 + lane * 16);
#pragma unroll
    for (int t = 0; t < 14; ++t)
#pragma unroll
      for (int sh = 0; sh < 2; ++sh)
        H[t][sh] = __builtin_amdgcn_raw_buffer_load_b128(rws, hi + sh * 1024, t * 2048, 16);
    SB();
#pragma unroll
    for (int t = 0; t < 14; ++t)
#pragma unroll
      for (int x = 0; x < 2; ++x) {
        u32x4 wf;
        if (t < 5) wf = W2R[t][x];
        else {
          const unsigned a = LB + w * 18432 + (w2l ^ (64u * x)) + (t - 5) * 2048;
          wf = trfrag(a, a + 1024);
        }
        acc[x][0] = mfma_bf(wf, H[t][0], acc[x][0]);
        acc[x][1] = mfma_bf(wf, H[t][1], acc[x][1]);
      }
  }
  {
    STAMP(17);
    const int rho = w & 1, kap = w >> 1;
    u32x4 I[8][2];
#pragma unroll
    for (int x = 0; x < 2; ++x) {
      const float d = (float)(64 * cg + 32 * x + l31);
      const float tw = 2.0f * cosr(rev448(8.0f * d));
      f32x2 f0[4], f1[4];
#pragma unroll
      for (int e = 0; e < 4; ++e) {
        const float kd = (float)(56 * kap + 4 * h + e);
        const float a0 = rev448(kd * d), a1 = rev448((kd + 8.0f) * d);
        f0[e >> 1][e & 1] = cosr(a0);
        f0[2 + (e >> 1)][e & 1] = sinr(-a0);
        f1[e >> 1][e & 1] = cosr(a1);
        f1[2 + (e >> 1)][e & 1] = sinr(-a1);
      }
#pragma unroll
      for (int i = 0; i < 8; ++i) {
#pragma unroll
        for (int e = 0; e < 4; ++e) {
          I[i][x][e] = pk_bf2(f0[e]);
          const f32x2 n = fma2(bc(tw), f1[e], -f0[e]);
          f0[e] = f1[e];
          f1[e] = n;
        }
        PIN(I[i][x]);
      }
    }
    u32x4 GC[2][2];
#pragma unroll
    for (int sh = 0; sh < 2; ++sh) {
      const float sf = (float)(32 * (32 * sh + l31) + s2) * (1.0f / 2048.0f);
#pragma unroll
      for (int t = 0; t < 2; ++t)
#pragma unroll
        for (int e = 0; e < 4; ++e) {
          const int p = 16 * rho + 8 * t + 4 * (e >> 1) + 2 * h + (e & 1);
          const float a = (float)p * sf;
          float sv = sinr(a);
          if (t == 0 && e == 0) sv = (p == 0) ? ((l31 & 1) ? -1.0f : 1.0f) : sv;
          GC[sh][t][e] = pk_bf(cosr(a), sv);
        }
      PIN(GC[sh][0]); PIN(GC[sh][1]);
    }
    STAMP(18);
    poll(sws, WS_AF + (unsigned)((4 * rho + (lane & 3)) * 4 + kap) * 128u, tag);
    STAMP(19);
    u32x4 A[8];
    const unsigned ai = WS_A + (unsigned)(((s2 * 2 + rho) * 29 + 7 * kap) * 1024 + lane * 16);
#pragma unroll
    for (int i = 0; i < 8; ++i)
      A[i] = __builtin_amdgcn_raw_buffer_load_b128(rws, (i == 7 && kap != 3) ? OOB : ai, i * 1024, 16);
    SB();
    f32x16 T[2] = {};
#pragma unroll
    for (int i = 0; i < 8; ++i) {
      T[0] = mfma_bf(A[i], I[i][0], T[0]);
      T[1] = mfma_bf(A[i], I[i][1], T[1]);
    }
#pragma unroll
    for (int x = 0; x < 2; ++x)
#pragma unroll
      for (int t = 0; t < 2; ++t) {
        u32x4 tf;
#pragma unroll
        for (int e = 0; e < 4; ++e) tf[e] = pk_bf(T[x][8 * t + 2 * e], T[x][8 * t + 2 * e + 1]);
        acc[x][0] = mfma_bf(tf, GC[0][t], acc[x][0]);
        acc[x][1] = mfma_bf(tf, GC[1][t], acc[x][1]);
      }
  }
  STAMP(20);
#pragma unroll
  for (int x = 0; x < 2; ++x)
#pragma unroll
    for (int sh = 0; sh < 2; ++sh)
#pragma unroll
      for (int q = 0; q < 4; ++q) {
        const f32x4 v = {acc[x][sh][4 * q], acc[x][sh][4 * q + 1], acc[x][sh][4 * q + 2], acc[x][sh][4 * q + 3]};
        *(LAS f32x4*)(LB + (unsigned)(w * 18432 + (32 * sh + l31) * 256 + (((8 * x + 2 * q + h) ^ (l31 & 7)) * 16))) = v;
      }
  STAMP(21);
  LBAR();
  STAMP(22);
  {
    const int s1 = tid >> 3, dc = tid & 7;
    f32x4 r0 = {}, r1 = {};
#pragma unroll
    for (int k = 0; k < 8; ++k) {
      r0 += *(LAS f32x4*)(LB + (unsigned)(k * 18432 + s1 * 256 + (((2 * dc) ^ (s1 & 7)) * 16)));
      r1 += *(LAS f32x4*)(LB + (unsigned)(k * 18432 + s1 * 256 + (((2 * dc + 1) ^ (s1 & 7)) * 16)));
    }
    u32x4 o;
    o[0] = pk_bf(r0[0] + bf_lo(xres[0]) + bf_lo(b2v[0]), r0[1] + bf_hi(xres[0]) + bf_hi(b2v[0]));
    o[1] = pk_bf(r0[2] + bf_lo(xres[1]) + bf_lo(b2v[1]), r0[3] + bf_hi(xres[1]) + bf_hi(b2v[1]));
    o[2] = pk_bf(r1[0] + bf_lo(xres[2]) + bf_lo(b2v[2]), r1[1] + bf_hi(xres[2]) + bf_hi(b2v[2]));
    o[3] = pk_bf(r1[2] + bf_lo(xres[3]) + bf_lo(b2v[3]), r1[3] + bf_hi(xres[3]) + bf_hi(b2v[3]));
    __builtin_amdgcn_raw_buffer_store_b128(o, rout, oo, 0, 0);
  }
  if (blockIdx.x == 0 && tid == 0) __builtin_amdgcn_raw_buffer_store_b64(tag, rws, WS_E, 0, 16);
  STAMP(23);
  STAMP_OUT();
}

extern "C" void kernel_launch(void* const* d_in, const int* in_sizes, int n_in, void* d_out, int out_size,
                              void* d_ws, size_t ws_size, hipStream_t stream) {
  fused_kernel<<<256, 512, 0, stream>>>(d_in[0], d_in[1], d_in[2], d_in[4], d_in[5], d_in[6], d_in[7], d_out, d_ws);
}
```

```cpp
#define PROBE -1
#include <hip/hip_runtime.h>
#include <stdint.h>
#pragma clang diagnostic ignored "-Wint-to-pointer-cast"

#define DEV __device__ __forceinline__
#define LAS __attribute__((address_space(3)))
typedef __attribute__((ext_vector_type(4))) short s16x4;
typedef __attribute__((ext_vector_type(8))) short s16x8;
typedef __attribute__((ext_vector_type(8))) __bf16 bf16x8;
typedef __attribute__((ext_vector_type(2))) __bf16 bf16x2;
typedef __attribute__((ext_vector_type(8))) _Float16 f16x8;
typedef __attribute__((ext_vector_type(2))) _Float16 f16x2;
typedef __attribute__((ext_vector_type(2))) float f32x2;
typedef __attribute__((ext_vector_type(4))) float f32x4;
typedef __attribute__((ext_vector_type(16))) float f32x16;
typedef __attribute__((ext_vector_type(2))) unsigned u32x2;
typedef __attribute__((ext_vector_type(4))) unsigned u32x4;
typedef __amdgpu_buffer_rsrc_t rsrc_t;

constexpr unsigned WS_E = 0;
constexpr unsigned WS_ZF = 4096;
constexpr unsigned WS_HF = WS_ZF + 224 * 128;
constexpr unsigned WS_AF = WS_HF + 224 * 128;
constexpr unsigned WS_Z = 65536;
constexpr unsigned WS_A = WS_Z + 29 * 16 * 4096;
constexpr unsigned WS_H = WS_A + 32 * 2 * 29 * 1024;
constexpr unsigned WS_END = WS_H + 32 * 112 * 2048;
constexpr unsigned OOB = 0x80000000u;
constexpr int SPIN_CAP = 1 << 16;

constexpr unsigned L_MISC = 147456;
constexpr unsigned L_STAT = L_MISC;
constexpr unsigned L_RROW = L_STAT + 4096;
constexpr unsigned L_CS = L_RROW + 1024;
constexpr unsigned L_GATH = 4 * 16384;
constexpr unsigned L_W2R1 = 4 * 16384;
DEV constexpr unsigned xblk(int kc) { return (unsigned)((kc + 8) % 9) * 16384u; }
DEV constexpr unsigned w1blk(int bi) { return (unsigned)((bi + 6) % 9) * 16384u; }
DEV constexpr unsigned w1row(int n) { return w1blk(n >> 7) + (unsigned)(n & 127) * 128u; }

#define WAITBAR(n) asm volatile("s_waitcnt vmcnt(" #n ") lgkmcnt(0)\n\ts_barrier" ::: "memory")
#define LBAR() asm volatile("s_waitcnt lgkmcnt(0)\n\ts_barrier" ::: "memory")
#define PIN(x) asm volatile("" : "+v"(x))
#define SB() __builtin_amdgcn_sched_barrier(0)
#define STAMP(i) do { if ((i) == PROBE) { SB(); probe_t = (unsigned)__builtin_amdgcn_s_memrealtime(); asm volatile("s_waitcnt lgkmcnt(0)" ::: "memory"); SB(); } if ((i) == 0 && tid == 0) __builtin_amdgcn_raw_buffer_store_b32(rt0, rws, 1024 + blockIdx.x * 4, 0, 16); } while (0)
#define STAMP_OUT() do { if (loc < 28) { u32x4 q_ = __builtin_amdgcn_raw_buffer_load_b128(rws, 1024 + lane * 16, 0, 16); \
    int m_ = min(min((int)(q_[0] - rt0), (int)(q_[1] - rt0)), min((int)(q_[2] - rt0), (int)(q_[3] - rt0))); \
    for (int o_ = 32; o_; o_ >>= 1) m_ = min(m_, __shfl_xor(m_, o_)); \
    const unsigned g_ = rt0 + (unsigned)__builtin_amdgcn_readfirstlane(m_); \
    unsigned lim_ = 4000u + (PROBE >= 0 ? 4u * (probe_t - g_) : 0u); if (lim_ > 20000u) lim_ = 20000u; \
    while ((unsigned)__builtin_amdgcn_s_memrealtime() - g_ < lim_) __builtin_amdgcn_s_sleep(1); } } while (0)
#define WAITVM(n) asm volatile("s_waitcnt vmcnt(" #n ")" ::: "memory")

DEV u32x4 mk_srd(const void* p, unsigned bytes) {
  uint64_t a = (uint64_t)p;
  u32x4 r = {(unsigned)a, (unsigned)(a >> 32) & 0xffffu, bytes, 0x00020000u};
  return r;
}
DEV void dma16(unsigned lds, unsigned voff, u32x4 srd, unsigned soff) {
  asm volatile("s_mov_b32 m0, %0\n\ts_nop 0\n\tbuffer_load_dwordx4 %1, %2, %3 offen lds"
               :: "s"(lds), "v"(voff), "s"(srd), "s"(soff) : "memory");
}
DEV u32x4 lds128(unsigned a) { return *(LAS u32x4*)a; }
DEV void lds128w(unsigned a, u32x4 v) { *(LAS u32x4*)a = v; }
DEV u32x4 trfrag(unsigned a0, unsigned a1) {
  s16x4 lo = __builtin_amdgcn_ds_read_tr16_b64_v4i16((LAS s16x4*)a0);
  s16x4 hi = __builtin_amdgcn_ds_read_tr16_b64_v4i16((LAS s16x4*)a1);
  return __builtin_bit_cast(u32x4, __builtin_shufflevector(lo, hi, 0, 1, 2, 3, 4, 5, 6, 7));
}
DEV f32x16 mfma_bf(u32x4 a, u32x4 b, f32x16 c) {
  return __builtin_amdgcn_mfma_f32_32x32x16_bf16(__builtin_bit_cast(bf16x8, a), __builtin_bit_cast(bf16x8, b), c, 0, 0, 0);
}
DEV f32x16 mfma_h(u32x4 a, u32x4 b, f32x16 c) {
  return __builtin_amdgcn_mfma_f32_32x32x16_f16(__builtin_bit_cast(f16x8, a), __builtin_bit_cast(f16x8, b), c, 0, 0, 0);
}
DEV unsigned pk_bf(float lo, float hi) {
  f32x2 p = {lo, hi};
  return __builtin_bit_cast(unsigned, __builtin_convertvector(p, bf16x2));
}
DEV unsigned pk_h(float lo, float hi) {
  f32x2 p = {lo, hi};
  return __builtin_bit_cast(unsigned, __builtin_convertvector(p, f16x2));
}
DEV unsigned pk_bf2(f32x2 p) { return __builtin_bit_cast(unsigned, __builtin_convertvector(p, bf16x2)); }
DEV unsigned pk_h2(f32x2 p) { return __builtin_bit_cast(unsigned, __builtin_convertvector(p, f16x2)); }
DEV f32x2 pr(const f32x16& v, int r) { return f32x2{v[r], v[r + 1]}; }
DEV f32x2 fma2(f32x2 a, f32x2 b, f32x2 c) { return __builtin_elementwise_fma(a, b, c); }
DEV f32x2 bc(float a) { return f32x2{a, a}; }
DEV float bf_lo(unsigned v) { return __builtin_bit_cast(float, v << 16); }
DEV float bf_hi(unsigned v) { return __builtin_bit_cast(float, v & 0xffff0000u); }
DEV float bf16r(float x) { return bf_lo(pk_bf(x, 0.f)); }
DEV float dot2(unsigned a, unsigned b, float c) {
  return __builtin_amdgcn_fdot2_f32_bf16(__builtin_bit_cast(bf16x2, a), __builtin_bit_cast(bf16x2, b), c, false);
}
DEV float cosr(float rev) { return __builtin_amdgcn_cosf(rev); }
DEV float sinr(float rev) { return __builtin_amdgcn_sinf(rev); }
DEV float rev448(float n) {
  float q = __builtin_floorf(n * (1.0f / 448.0f));
  return __builtin_fmaf(q, -448.0f, n) * (1.0f / 448.0f);
}
DEV void poll(u32x4 srd, unsigned voff, u32x2 tag) {
#pragma unroll 1
  for (int n = 0; n < SPIN_CAP; ++n) {
    u32x2 v;
    asm volatile("buffer_load_dwordx2 %0, %1, %2, 0 offen sc1\n\ts_waitcnt vmcnt(0)"
                 : "=&v"(v) : "v"(voff), "s"(srd) : "memory");
    if (__builtin_amdgcn_ballot_w64(v[0] != tag[0] || v[1] != tag[1]) == 0) break;
    __builtin_amdgcn_s_sleep(1);
  }
}
DEV unsigned sel_x1(bool keep, unsigned own, unsigned other) {
  unsigned o = __builtin_amdgcn_update_dpp(0u, other, 0xB1, 0xf, 0xf, true);
  return keep ? own : o;
}
DEV unsigned sel_x2(bool keep, unsigned own, unsigned other) {
  unsigned o = __builtin_amdgcn_update_dpp(0u, other, 0x4E, 0xf, 0xf, true);
  return keep ? own : o;
}

__global__ void __launch_bounds__(512, 2)
fused_kernel(const void* __restrict__ px, const void* __restrict__ pwr, const void* __restrict__ pwi,
             const void* __restrict__ pw1, const void* __restrict__ pb1, const void* __restrict__ pw2,
             const void* __restrict__ pb2, void* __restrict__ pout, void* __restrict__ pws) {
  __shared__ __attribute__((aligned(1024))) char smem[163840];
  const unsigned LB = (unsigned)(uintptr_t)(LAS char*)smem;
  const int tid = threadIdx.x;
  const int lane = tid & 63;
  const int w = __builtin_amdgcn_readfirstlane(tid >> 6);
  const int l31 = lane & 31, h = lane >> 5;
  const int xcd = blockIdx.x & 7, loc = blockIdx.x >> 3;

  const u32x4 sws = mk_srd(pws, WS_END);
  const rsrc_t rws = __builtin_amdgcn_make_buffer_rsrc(pws, 0, WS_END, 0x00020000);

  const unsigned rt0 = (unsigned)__builtin_amdgcn_s_memrealtime();
  unsigned probe_t = 0;
  STAMP(0);
  u32x2 ev;
  asm volatile("s_nop 4\n\tbuffer_load_dwordx2 %0, off, %1, 0 sc1" : "=v"(ev) : "s"(sws) : "memory");

  if (loc >= 28) {
    const int pq = xcd, qt = loc - 28;
    const bool active = (w < 7) || (qt == 3);
    const int g = (w < 7) ? 7 * qt + w : 28;
    const int kdl = lane & 7, pl = (lane >> 3) & 3;
    const int p = 4 * pq + pl, kd = 8 * g + kdl;
    WAITVM(0);
    asm volatile("" : "+v"(ev));
    uint64_t e64 = ((uint64_t)__builtin_amdgcn_readfirstlane(ev[1]) << 32) | (unsigned)__builtin_amdgcn_readfirstlane(ev[0]);
    e64 = e64 * 6364136223846793005ull + 1442695040888963407ull;
    const u32x2 tag = {(unsigned)e64, (unsigned)(e64 >> 32)};
    if (active) {
      const rsrc_t rwr = __builtin_amdgcn_make_buffer_rsrc((void*)pwr, 0, 2048 * 225 * 2, 0x00020000);
      const rsrc_t rwi = __builtin_amdgcn_make_buffer_rsrc((void*)pwi, 0, 2048 * 225 * 2, 0x00020000);
      f32x2 wpr[8], wpi[8], wmr[8], wmi[8];
      const float wsc = ((kd == 0 || kd == 224) ? 1.0f : 2.0f) / (2048.0f * 448.0f);
#pragma unroll
      for (int r = 0; r < 16; ++r) {
        const int k2 = (r & 3) + 8 * (r >> 2) + 4 * h;
        const int kp = p + 64 * k2;
        const int km = p ? ((2048 - p + 64 * k2) & 2047) : (32 + 64 * k2);
        const unsigned op = kd > 224 ? OOB : (unsigned)(kp * 225 + kd) * 2u;
        const unsigned om = kd > 224 ? OOB : (unsigned)(km * 225 + kd) * 2u;
        wpr[r >> 1][r & 1] = bf_lo(__builtin_amdgcn_raw_buffer_load_b16(rwr, op, 0, 0));
        wpi[r >> 1][r & 1] = bf_lo(__builtin_amdgcn_raw_buffer_load_b16(rwi, op, 0, 0));
        wmr[r >> 1][r & 1] = bf_lo(__builtin_amdgcn_raw_buffer_load_b16(rwr, om, 0, 0));
        wmi[r >> 1][r & 1] = bf_lo(__builtin_amdgcn_raw_buffer_load_b16(rwi, om, 0, 0));
      }
      u32x4 FR[4], FI[4], IR[4], II[4];
#pragma unroll
      for (int t = 0; t < 4; ++t) {
        float c[4], sn[4];
#pragma unroll
        for (int e = 0; e < 4; ++e) {
          const float a = (float)(l31 * (8 * t + 4 * h + e)) * (1.0f / 32.0f);
          c[e] = cosr(a);
          sn[e] = sinr(a);
          FR[t][e] = pk_h(c[e], sn[e]);
          FI[t][e] = pk_h(-sn[e], c[e]);
        }
        IR[t] = u32x4{pk_bf(c[0], c[1]), pk_bf(c[2], c[3]), pk_bf(-sn[0], -sn[1]), pk_bf(-sn[2], -sn[3])};
        II[t] = u32x4{pk_bf(sn[0], sn[1]), pk_bf(sn[2], sn[3]), pk_bf(c[0], c[1]), pk_bf(c[2], c[3])};
        PIN(FR[t]); PIN(FI[t]); PIN(IR[t]); PIN(II[t]);
      }
      float ff = pq ? 1.0f : 0.0f;
      float cph = pq ? 1.0f : cosr((float)l31 * (1.0f / 64.0f));
      float sph = pq ? 0.0f : sinr((float)l31 * (1.0f / 64.0f));
      PIN(ff); PIN(cph); PIN(sph);
#pragma unroll
      for (int r = 0; r < 8; ++r) {
        wpr[r] *= bc(wsc); wpi[r] *= bc(wsc); wmr[r] *= bc(wsc); wmi[r] *= bc(wsc);
        PIN(wpr[r]); PIN(wpi[r]); PIN(wmr[r]); PIN(wmi[r]);
      }
      unsigned zo = (kd > 224) ? OOB : WS_Z + (unsigned)((g * 16 + 2 * h) * 4096 + p * 128 + kdl * 16);
      unsigned ao = WS_A + (unsigned)((((lane & 28) * 2 + (pq >> 2)) * 29 + g) * 1024 + (32 * h + 8 * (pq & 3) + (lane & 3)) * 16);
      PIN(zo); PIN(ao);
      STAMP(1);
      const int jz = (g == 28) ? 0 : (g >> 1);
      poll(sws, WS_ZF + (unsigned)((lane & 15) * 14 + jz) * 128u, tag);
      STAMP(2);

      u32x4 z[8];
#pragma unroll
      for (int i = 0; i < 8; ++i)
        z[i] = __builtin_amdgcn_raw_buffer_load_b128(rws, zo, (4 * (i >> 1) + (i & 1)) * 4096, 16);
      SB();
      f32x16 xpr = {}, xpi = {}, xmr = {}, xmi = {};
#pragma unroll
      for (int t = 0; t < 4; ++t) {
        const u32x4 zp = {z[2 * t][0], z[2 * t][1], z[2 * t + 1][0], z[2 * t + 1][1]};
        const u32x4 zm = {z[2 * t][2], z[2 * t][3], z[2 * t + 1][2], z[2 * t + 1][3]};
        xpr = mfma_h(FR[t], zp, xpr);
        xpi = mfma_h(FI[t], zp, xpi);
        xmr = mfma_h(FR[t], zm, xmr);
        xmi = mfma_h(FI[t], zm, xmi);
      }
      STAMP(3);
      f32x16 ypr = {}, ypi = {}, ymr = {}, ymi = {};
#pragma unroll
      for (int t = 0; t < 4; ++t) {
        u32x4 pp, pm;
#pragma unroll
        for (int e = 0; e < 2; ++e) {
          const int r = 4 * t + 2 * e, q = 2 * t + e;
          pp[e] = pk_bf2(fma2(pr(xpr, r), wpr[q], -(pr(xpi, r) * wpi[q])));
          pp[2 + e] = pk_bf2(fma2(pr(xpr, r), wpi[q], pr(xpi, r) * wpr[q]));
          pm[e] = pk_bf2(fma2(pr(xmr, r), wmr[q], -(pr(xmi, r) * wmi[q])));
          pm[2 + e] = pk_bf2(fma2(pr(xmr, r), wmi[q], pr(xmi, r) * wmr[q]));
        }
        ypr = mfma_bf(pp, IR[t], ypr);
        ypi = mfma_bf(pp, II[t], ypi);
        ymr = mfma_bf(pm, IR[t], ymr);
        ymi = mfma_bf(pm, II[t], ymi);
      }
      STAMP(4);
      u32x4 un[8];
#pragma unroll
      for (int r = 0; r < 16; r += 2) {
        f32x2 acr, aci, asr, asi;
        if (r < 4) {
          acr = fma2(bc(ff), pr(ymr, r), pr(ypr, r));
          aci = fma2(bc(ff), pr(ymi, r), pr(ypi, r));
          asr = fma2(bc(cph), pr(ymi, r), fma2(bc(sph), pr(ymr, r), -(bc(ff) * pr(ypi, r))));
          asi = fma2(bc(sph), pr(ymi, r), fma2(bc(ff), pr(ypr, r), -(bc(cph) * pr(ymr, r))));
        } else {
          acr = pr(ypr, r) + pr(ymr, r);
          aci = pr(ypi, r) + pr(ymi, r);
          asr = pr(ymi, r) - pr(ypi, r);
          asi = pr(ypr, r) - pr(ymr, r);
        }
        un[2 * (r >> 2)][(r >> 1) & 1] = pk_bf2(acr);
        un[2 * (r >> 2)][2 + ((r >> 1) & 1)] = pk_bf2(aci);
        un[2 * (r >> 2) + 1][(r >> 1) & 1] = pk_bf2(asr);
        un[2 * (r >> 2) + 1][2 + ((r >> 1) & 1)] = pk_bf2(asi);
      }
      const bool b0 = lane & 1, b1 = lane & 2;
#pragma unroll
      for (int st = 0; st < 2; ++st) {
        u32x4 a[4], b[4];
#pragma unroll
        for (int e = 0; e < 4; ++e) {
          a[0][e] = sel_x1(!b0, un[4 * st + 0][e], un[4 * st + 1][e]);
          a[1][e] = sel_x1(b0, un[4 * st + 1][e], un[4 * st + 0][e]);
          a[2][e] = sel_x1(!b0, un[4 * st + 2][e], un[4 * st + 3][e]);
          a[3][e] = sel_x1(b0, un[4 * st + 3][e], un[4 * st + 2][e]);
          b[0][e] = sel_x2(!b1, a[0][e], a[2][e]);
          b[2][e] = sel_x2(b1, a[2][e], a[0][e]);
          b[1][e] = sel_x2(!b1, a[1][e], a[3][e]);
          b[3][e] = sel_x2(b1, a[3][e], a[1][e]);
        }
#pragma unroll
        for (int t = 0; t < 4; ++t)
          __builtin_amdgcn_raw_buffer_store_b128(b[t], rws, ao + 64 * st + t * 2 * 29 * 1024, 0, 16);
      }
    }
    STAMP(5);
    WAITBAR(0);
    if (tid == 0) __builtin_amdgcn_raw_buffer_store_b64(tag, rws, WS_AF + (unsigned)(pq * 4 + qt) * 128u, 0, 16);
    STAMP(6);
    STAMP_OUT();
    return;
  }

  const int rg = 4 * (xcd >> 1) + loc / 7, j = 7 * (xcd & 1) + loc % 7;
  const int s2 = 4 * xcd + loc / 7, cg = loc % 7;
  const int rgw = w & 3, cgw = w >> 2;
  const u32x4 sx = mk_srd(px, 2048 * 448 * 2), sw1 = mk_srd(pw1, 448 * 1792 * 2), sw2 = mk_srd(pw2, 448 * 1792 * 2);
  const u32x4 sb1 = mk_srd(pb1, 1792 * 2);
  unsigned b1v[2];
  {
    const unsigned o = h ? OOB : (unsigned)(128 * j + 32 * cgw + l31) * 2u;
    asm volatile("buffer_load_ushort %0, %2, %3, 0 offen\n\tbuffer_load_ushort %1, %2, %3, 0 offen offset:128"
                 : "=&v"(b1v[0]), "=&v"(b1v[1]) : "v"(o), "s"(sb1) : "memory");
  }
  {
    const int rho = 8 * w + (lane >> 3);
    const unsigned vx = (unsigned)((32 * rho + 2 * rg) * 896 + (((lane & 7) ^ ((rho >> 1) & 7)) * 16));
#pragma unroll
    for (int kc = 0; kc < 7; ++kc) {
      dma16(LB + xblk(kc) + w * 1024, vx, sx, kc * 128);
      dma16(LB + xblk(kc) + 8192 + w * 1024, vx, sx, kc * 128 + 896);
    }
  }
  const unsigned swz16 = (unsigned)(((lane & 7) ^ ((lane >> 2) & 4)) * 16);
  const unsigned vw1 = (unsigned)(lane >> 3) * 3584u + swz16;
  auto w1dma = [&](int bi) {
#pragma unroll
    for (int q = 0; q < 2; ++q) {
      const int n0 = 128 * bi + 64 * q + 8 * w;
      const int so = (n0 >= 448) ? (n0 - 448) * 3584 + 128 : n0 * 3584;
      dma16(LB + w1blk(bi) + q * 8192 + w * 1024, vw1, sw1, so + 256 * j);
    }
  };
  w1dma(0);
  w1dma(1);
  STAMP(1);

  u32x4 F[14];
  u32x4 faug;
  {
    const int c = l31 & 15, uv = l31 >> 4;
    const bool sp = (j == 0) && (l31 == 16);
    const float kdf = sp ? 224.0f : (float)(16 * j + c);
    const float ph = (uv && !sp) ? 0.25f : 0.0f;
    const float tw = 2.0f * cosr(rev448(32.0f * kdf));
    f32x2 f0[4], f1[4];
#pragma unroll
    for (int e = 0; e < 8; ++e) {
      const float d = (float)(16 * cgw + 8 * h + e);
      f0[e >> 1][e & 1] = cosr(rev448(d * kdf) + ph);
      f1[e >> 1][e & 1] = cosr(rev448((d + 32.0f) * kdf) + ph);
    }
    float cs = 0.f;
#pragma unroll
    for (int i = 0; i < 14; ++i) {
#pragma unroll
      for (int e = 0; e < 4; ++e) {
        F[i][e] = pk_bf2(f0[e]);
        cs = dot2(F[i][e], 0x3f803f80u, cs);
        const f32x2 n = fma2(bc(tw), f1[e], -f0[e]);
        f0[e] = f1[e];
        f1[e] = n;
      }
      PIN(F[i]);
    }
    const float hi = bf16r(cs);
    faug = u32x4{pk_bf(hi, cs - hi), 0u, 0u, 0u};
    PIN(faug);
  }
  u32x4 GA[2][2];
  {
    const int hh = (l31 >> 2) & 1, reg = (l31 & 3) + 4 * (l31 >> 3);
    const int s2a = 2 * rg + (rgw >> 1);
#pragma unroll
    for (int mt = 0; mt < 2; ++mt) {
      const int p = 16 * mt + 8 * hh + (reg >> 1), cs = reg & 1;
      const bool sp = (p == 0) && cs;
      const float ca = sp ? 0.5f : (float)p * (1.0f / 64.0f);
      const float cb = sp ? 0.0f : (float)(p * s2a) * (1.0f / 2048.0f) - 0.25f * (float)cs;
#pragma unroll
      for (int t = 0; t < 2; ++t)
#pragma unroll
        for (int e = 0; e < 4; ++e) {
          const float s1 = (float)(32 * (rgw & 1) + 16 * t + 8 * (e >> 1) + 4 * h + 2 * (e & 1));
          GA[mt][t][e] = pk_h(cosr(__builtin_fmaf(ca, s1, cb)), cosr(__builtin_fmaf(ca, s1 + 1.0f, cb)));
        }
      PIN(GA[mt][0]); PIN(GA[mt][1]);
    }
  }

  STAMP(2);
  u32x4 X[28];
  f32x16 U = {};
  float sum = 0.f, sq = 0.f;
  {
    const unsigned xr = LB + (unsigned)(32 * rgw + l31) * 128u;
    const unsigned xs = (unsigned)((2 * cgw + h) ^ ((l31 >> 1) & 7));
#define XSTEP(kc, n)                                                          \
    WAITBAR(n);                                                               \
    if (kc >= 1 && kc <= 5) w1dma(kc + 1);                                    \
    _Pragma("unroll") for (int q = 0; q < 4; ++q)                             \
      X[4 * kc + q] = lds128(xr + ((xs ^ (2 * q)) * 16) + xblk(kc));          \
    _Pragma("unroll") for (int q = 0; q < 4; q += 2) {                        \
      U = mfma_bf(X[4 * kc + q], F[2 * kc + (q >> 1)], U);                    \
      _Pragma("unroll") for (int e = 0; e < 4; ++e) {                         \
        sum = dot2(X[4 * kc + q][e], 0x3f803f80u, sum);                       \
        sq = dot2(X[4 * kc + q][e], X[4 * kc + q][e], sq);                    \
      }                                                                       \
    }
    XSTEP(0, 16) STAMP(3); XSTEP(1, 14) XSTEP(2, 14) XSTEP(3, 14) XSTEP(4, 14) XSTEP(5, 14) XSTEP(6, 14)
#undef XSTEP
  }
  asm volatile("" : "+v"(ev), "+v"(b1v[0]), "+v"(b1v[1]));
  uint64_t e64 = ((uint64_t)__builtin_amdgcn_readfirstlane(ev[1]) << 32) | (unsigned)__builtin_amdgcn_readfirstlane(ev[0]);
  e64 = e64 * 6364136223846793005ull + 1442695040888963407ull;
  const u32x2 tag = {(unsigned)e64, (unsigned)(e64 >> 32)};

  *(LAS f32x2*)(LB + L_STAT + (unsigned)((32 * rgw + l31) * 4 + 2 * cgw + h) * 8u) = f32x2{sum, sq};
  STAMP(4);
  LBAR();
  float sig, rstd;
  u32x4 xaug;
  {
    const f32x4 a = *(LAS f32x4*)(LB + L_STAT + (unsigned)(32 * rgw + l31) * 32u);
    const f32x4 b = *(LAS f32x4*)(LB + L_STAT + (unsigned)(32 * rgw + l31) * 32u + 16u);
    const float mean = ((a[0] + a[2]) + (b[0] + b[2])) * (1.0f / 448.0f);
    const float ex2 = ((a[1] + a[3]) + (b[1] + b[3])) * (1.0f / 448.0f);
    const float var = __builtin_fmaxf(__builtin_fmaf(-mean, mean, ex2), 0.0f);
    sig = bf16r(__builtin_amdgcn_sqrtf(bf16r(bf16r(var) + 1e-5f)));
    rstd = 1.0f / sig;
    xaug = u32x4{pk_bf(-mean, -mean), pk_bf(sig, 0.f), 0u, 0u};
  }
  *(LAS float*)(LB + L_RROW + (unsigned)(w * 32 + l31) * 4u) = rstd;
  U = mfma_bf(xaug, faug, U);
  {
    u32x4 ub[2];
#pragma unroll
    for (int q = 0; q < 4; ++q) {
      const f32x4 rr = *(LAS f32x4*)(LB + L_RROW + (unsigned)(w * 32 + 8 * q + 4 * h) * 4u);
      ub[q >> 1][2 * (q & 1)] = pk_h2(pr(U, 4 * q) * f32x2{rr[0], rr[1]});
      ub[q >> 1][2 * (q & 1) + 1] = pk_h2(pr(U, 4 * q + 2) * f32x2{rr[2], rr[3]});
    }
#pragma unroll
    for (int mt = 0; mt < 2; ++mt) {
      f32x16 za = {};
      za = mfma_h(GA[mt][0], ub[0], za);
      za = mfma_h(GA[mt][1], ub[1], za);
#pragma unroll
      for (int q = 0; q < 2; ++q) {
        u32x4 v;
#pragma unroll
        for (int e = 0; e < 4; ++e) v[e] = pk_h(za[8 * q + 2 * e], za[8 * q + 2 * e + 1]);
        lds128w(LB + L_GATH + (unsigned)(w * 4096 + l31 * 128 + (((4 * mt + 2 * h + q) ^ (l31 & 7)) * 16)), v);
      }
    }
  }
  STAMP(5);
  WAITBAR(10);
  STAMP(6);
  {
    const int gl = tid >> 8, p = (tid >> 3) & 31, kdl = tid & 7;
    const unsigned ga = LB + L_GATH + (unsigned)((8 * gl + kdl) * 128 + (((p >> 2) ^ kdl) * 16) + (p & 3) * 4);
    const bool sp = (j == 0) && (gl == 0) && (kdl == 0);
    const float ff = p ? 1.0f : 0.0f;
    u32x4 z, zx;
#pragma unroll
    for (int sg = 0; sg < 2; ++sg) {
      float v[4];
#pragma unroll
      for (int uv = 0; uv < 2; ++uv) {
        f16x2 t[4];
#pragma unroll
        for (int k = 0; k < 4; ++k)
          t[k] = *(LAS f16x2*)(ga + (unsigned)((2 * sg + (k & 1) + 4 * (k >> 1)) * 4096 + uv * 2048));
        const f16x2 ts = (t[0] + t[1]) + (t[2] + t[3]);
        v[2 * uv] = (float)ts[0];
        v[2 * uv + 1] = (float)ts[1];
      }
      const float ang = (float)(2 * rg + sg) * (1.0f / 64.0f);
      const float cph = p ? 1.0f : cosr(ang), sph = p ? 0.0f : sinr(ang);
      const float a = v[0], e = v[1], c = sp ? 0.f : v[2], b = sp ? 0.f : v[3];
      float o[8] = {a + ff * b, c - ff * e, ff * a - (cph * b - sph * e), ff * c + (cph * e + sph * b),
                    v[2], -ff * v[3], ff * v[2] + sph * v[3], cph * v[3]};
#pragma unroll
      for (int k = 0; k < 8; ++k) asm("" : "+v"(o[k]));
      z[sg] = pk_h(o[0], o[1]);
      z[2 + sg] = pk_h(o[2], o[3]);
      zx[sg] = pk_h(o[4], o[5]);
      zx[2 + sg] = pk_h(o[6], o[7]);
    }
    __builtin_amdgcn_raw_buffer_store_b128(z, rws, WS_Z + (unsigned)(((2 * j + gl) * 16 + rg) * 4096 + (tid & 255) * 16), 0, 16);
    __builtin_amdgcn_raw_buffer_store_b128(zx, rws, sp ? WS_Z + (unsigned)((28 * 16 + rg) * 4096 + p * 128) : OOB, 0, 16);
  }

  SB();
  STAMP(7);
  const int tq = (lane >> 2) & 3, tp = lane & 3, tg = (lane >> 4) & 1;
  const unsigned w1b = LB + (unsigned)((8 * h + tq) * 128 + 64 * (cgw ^ (tq >> 1)) + 32 * tg + 8 * tp);
  const unsigned w1e = w1b + 2048u * cgw, w1o = w1b + 2048u * (1 - cgw);
  auto w1frag = [&](int c, int u) {
    const unsigned a = ((u & 1) ? w1o : w1e) + w1row(448 * c + 16 * (u & ~1));
    return trfrag(a, a + 512);
  };
  auto colsum = [&](int c) {
    float cs = 0.f;
#pragma unroll
    for (int i = 0; i < 7; ++i) {
      const unsigned a = w1b + 2048u * rgw + w1row(448 * c + 64 * i);
      const u32x4 f = trfrag(a, a + 512);
#pragma unroll
      for (int e = 0; e < 4; ++e) cs = dot2(f[e], 0x3f803f80u, cs);
    }
    *(LAS float*)(LB + L_CS + (unsigned)(((c * 2 + cgw) * 64 + lane) * 4 + rgw) * 4u) = cs;
  };
  const float gc1 = -2.302208198f * rstd, gc2 = gc1 * 0.044715f * rstd * rstd;
  const unsigned ho = WS_H + (unsigned)((((2 * rg + (rgw >> 1)) * 112 + 8 * j + 2 * cgw) * 2 + (rgw & 1)) * 1024 + lane * 16);
  auto finish = [&](int c, f32x16 acc) {
    const f32x4 pc = *(LAS f32x4*)(LB + L_CS + (unsigned)((c * 2 + cgw) * 64 + lane) * 16u);
    const float cs = ((pc[0] + pc[1]) + pc[2]) + pc[3];
    const float hi = bf16r(cs);
    const u32x4 waug = {pk_bf(hi, cs - hi), b1v[c], 0u, 0u};
    acc = mfma_bf(waug, xaug, acc);
#pragma unroll
    for (int t = 0; t < 2; ++t) {
      u32x4 o;
#pragma unroll
      for (int e = 0; e < 4; ++e) {
        const f32x2 u = pr(acc, 8 * t + 2 * e);
        const f32x2 ar = fma2(u * u, bc(gc2), bc(gc1)) * u;
        const f32x2 ex = {__builtin_amdgcn_exp2f(ar[0]), __builtin_amdgcn_exp2f(ar[1])};
        const f32x2 dn = fma2(ex, bc(sig), bc(sig));
        o[e] = pk_bf2(u * f32x2{__builtin_amdgcn_rcpf(dn[0]), __builtin_amdgcn_rcpf(dn[1])});
      }
      __builtin_amdgcn_raw_buffer_store_b128(o, rws, ho + t * 2048 + c * 8192, 0, 16);
    }
  };
  f32x16 h0 = {}, h1 = {};
#pragma unroll
  for (int u = 0; u < 16; ++u) h0 = mfma_bf(w1frag(0, u), X[u], h0);
  STAMP(8);
  WAITBAR(8);
  colsum(0);
#pragma unroll
  for (int u = 16; u < 28; ++u) h0 = mfma_bf(w1frag(0, u), X[u], h0);
  STAMP(9);
  WAITBAR(0);
  STAMP(10);
  if (tid == 0) __builtin_amdgcn_raw_buffer_store_b64(tag, rws, WS_ZF + (unsigned)(rg * 14 + j) * 128u, 0, 16);
  const unsigned vw2 = (unsigned)(lane >> 3) * 896u + swz16;
  const unsigned sw2b = (unsigned)(224 * w * 896 + 128 * cg);
#pragma unroll
  for (int pc = 0; pc < 10; ++pc) dma16(LB + L_W2R1 + w * 10240 + pc * 1024, vw2, sw2, sw2b + pc * 7168);
  colsum(1);
  finish(0, h0);
#pragma unroll
  for (int u = 0; u < 28; ++u) h1 = mfma_bf(w1frag(1, u), X[u], h1);
  STAMP(11);
  WAITVM(2);
  const unsigned w2l = (unsigned)((4 * h + tq) * 128 + 64 * (tq >> 1) + 32 * tg + 8 * tp);
  u32x4 W2R[5][2];
#pragma unroll
  for (int t = 0; t < 5; ++t)
#pragma unroll
    for (int x = 0; x < 2; ++x) {
      const unsigned a = LB + L_W2R1 + w * 10240 + (w2l ^ (64u * x)) + t * 2048;
      W2R[t][x] = trfrag(a, a + 1024);
    }
  LBAR();
  STAMP(12);
  finish(1, h1);
  STAMP(13);
#pragma unroll
  for (int pc = 10; pc < 28; ++pc) dma16(LB + w * 18432 + (pc - 10) * 1024, vw2, sw2, sw2b + pc * 7168);
  STAMP(14);
  WAITBAR(18);
  STAMP(15);
  if (tid == 0) __builtin_amdgcn_raw_buffer_store_b64(tag, rws, WS_HF + (unsigned)(rg * 14 + j) * 128u, 0, 16);

  const rsrc_t rx = __builtin_amdgcn_make_buffer_rsrc((void*)px, 0, 2048 * 448 * 2, 0x00020000);
  const rsrc_t rb2 = __builtin_amdgcn_make_buffer_rsrc((void*)pb2, 0, 448 * 2, 0x00020000);
  const rsrc_t rout = __builtin_amdgcn_make_buffer_rsrc(pout, 0, 2048 * 448 * 2, 0x00020000);
  const unsigned oo = (unsigned)((32 * (tid >> 3) + s2) * 896 + 128 * cg + (tid & 7) * 16);
  const u32x4 xres = __builtin_amdgcn_raw_buffer_load_b128(rx, oo, 0, 0);
  const u32x4 b2v = __builtin_amdgcn_raw_buffer_load_b128(rb2, (unsigned)(128 * cg + (tid & 7) * 16), 0, 0);
  {
    const int j0 = (224 * w) >> 7, j1 = (224 * w + 223) >> 7;
    const int jj = min(j0 + (lane & 3), j1);
    poll(sws, WS_HF + (unsigned)((s2 >> 1) * 14 + jj) * 128u, tag);
  }
  STAMP(16);
  f32x16 acc[2][2] = {};
  {
    u32x4 H[14][2];
    const unsigned hi = WS_H + (unsigned)((s2 * 112 + 14 * w) * 2048 + lane * 16);
#pragma unroll
    for (int t = 0; t < 14; ++t)
#pragma unroll
      for (int sh = 0; sh < 2; ++sh)
        H[t][sh] = __builtin_amdgcn_raw_buffer_load_b128(rws, hi + sh * 1024, t * 2048, 16);
    SB();
#pragma unroll
    for (int t = 0; t < 14; ++t)
#pragma unroll
      for (int x = 0; x < 2; ++x) {
        u32x4 wf;
        if (t < 5) wf = W2R[t][x];
        else {
          const unsigned a = LB + w * 18432 + (w2l ^ (64u * x)) + (t - 5) * 2048;
          wf = trfrag(a, a + 1024);
        }
        acc[x][0] = mfma_bf(wf, H[t][0], acc[x][0]);
        acc[x][1] = mfma_bf(wf, H[t][1], acc[x][1]);
      }
  }
  {
    STAMP(17);
    const int rho = w & 1, kap = w >> 1;
    u32x4 I[8][2];
#pragma unroll
    for (int x = 0; x < 2; ++x) {
      const float d = (float)(64 * cg + 32 * x + l31);
      const float tw = 2.0f * cosr(rev448(8.0f * d));
      f32x2 f0[4], f1[4];
#pragma unroll
      for (int e = 0; e < 4; ++e) {
        const float kd = (float)(56 * kap + 4 * h + e);
        const float a0 = rev448(kd * d), a1 = rev448((kd + 8.0f) * d);
        f0[e >> 1][e & 1] = cosr(a0);
        f0[2 + (e >> 1)][e & 1] = sinr(-a0);
        f1[e >> 1][e & 1] = cosr(a1);
        f1[2 + (e >> 1)][e & 1] = sinr(-a1);
      }
#pragma unroll
      for (int i = 0; i < 8; ++i) {
#pragma unroll
        for (int e = 0; e < 4; ++e) {
          I[i][x][e] = pk_bf2(f0[e]);
          const f32x2 n = fma2(bc(tw), f1[e], -f0[e]);
          f0[e] = f1[e];
          f1[e] = n;
        }
        PIN(I[i][x]);
      }
    }
    u32x4 GC[2][2];
#pragma unroll
    for (int sh = 0; sh < 2; ++sh) {
      const float sf = (float)(32 * (32 * sh + l31) + s2) * (1.0f / 2048.0f);
#pragma unroll
      for (int t = 0; t < 2; ++t)
#pragma unroll
        for (int e = 0; e < 4; ++e) {
          const int p = 16 * rho + 8 * t + 4 * (e >> 1) + 2 * h + (e & 1);
          const float a = (float)p * sf;
          float sv = sinr(a);
          if (t == 0 && e == 0) sv = (p == 0) ? ((l31 & 1) ? -1.0f : 1.0f) : sv;
          GC[sh][t][e] = pk_bf(cosr(a), sv);
        }
      PIN(GC[sh][0]); PIN(GC[sh][1]);
    }
    STAMP(18);
    poll(sws, WS_AF + (unsigned)((4 * rho + (lane & 3)) * 4 + kap) * 128u, tag);
    STAMP(19);
    u32x4 A[8];
    const unsigned ai = WS_A + (unsigned)(((s2 * 2 + rho) * 29 + 7 * kap) * 1024 + lane * 16);
#pragma unroll
    for (int i = 0; i < 8; ++i)
      A[i] = __builtin_amdgcn_raw_buffer_load_b128(rws, (i == 7 && kap != 3) ? OOB : ai, i * 1024, 16);
    SB();
    f32x16 T[2] = {};
#pragma unroll
    for (int i = 0; i < 8; ++i) {
      T[0] = mfma_bf(A[i], I[i][0], T[0]);
      T[1] = mfma_bf(A[i], I[i][1], T[1]);
    }
#pragma unroll
    for (int x = 0; x < 2; ++x)
#pragma unroll
      for (int t = 0; t < 2; ++t) {
        u32x4 tf;
#pragma unroll
        for (int e = 0; e < 4; ++e) tf[e] = pk_bf(T[x][8 * t + 2 * e], T[x][8 * t + 2 * e + 1]);
        acc[x][0] = mfma_bf(tf, GC[0][t], acc[x][0]);
        acc[x][1] = mfma_bf(tf, GC[1][t], acc[x][1]);
      }
  }
  STAMP(20);
#pragma unroll
  for (int x = 0; x < 2; ++x)
#pragma unroll
    for (int sh = 0; sh < 2; ++sh)
#pragma unroll
      for (int q = 0; q < 4; ++q) {
        const f32x4 v = {acc[x][sh][4 * q], acc[x][sh][4 * q + 1], acc[x][sh][4 * q + 2], acc[x][sh][4 * q + 3]};
        *(LAS f32x4*)(LB + (unsigned)(w * 18432 + (32 * sh + l31) * 256 + (((8 * x + 2 * q + h) ^ (l31 & 7)) * 16))) = v;
      }
  STAMP(21);
  LBAR();
  STAMP(22);
  {
    const int s1 = tid >> 3, dc = tid & 7;
    f32x4 r0 = {}, r1 = {};
#pragma unroll
    for (int k = 0; k < 8; ++k) {
      r0 += *(LAS f32x4*)(LB + (unsigned)(k * 18432 + s1 * 256 + (((2 * dc) ^ (s1 & 7)) * 16)));
      r1 += *(LAS f32x4*)(LB + (unsigned)(k * 18432 + s1 * 256 + (((2 * dc + 1) ^ (s1 & 7)) * 16)));
    }
    u32x4 o;
    o[0] = pk_bf(r0[0] + bf_lo(xres[0]) + bf_lo(b2v[0]), r0[1] + bf_hi(xres[0]) + bf_hi(b2v[0]));
    o[1] = pk_bf(r0[2] + bf_lo(xres[1]) + bf_lo(b2v[1]), r0[3] + bf_hi(xres[1]) + bf_hi(b2v[1]));
    o[2] = pk_bf(r1[0] + bf_lo(xres[2]) + bf_lo(b2v[2]), r1[1] + bf_hi(xres[2]) + bf_hi(b2v[2]));
    o[3] = pk_bf(r1[2] + bf_lo(xres[3]) + bf_lo(b2v[3]), r1[3] + bf_hi(xres[3]) + bf_hi(b2v[3]));
    __builtin_amdgcn_raw_buffer_store_b128(o, rout, oo, 0, 0);
  }
  if (blockIdx.x == 0 && tid == 0) __builtin_amdgcn_raw_buffer_store_b64(tag, rws, WS_E, 0, 16);
  STAMP(23);
  STAMP_OUT();
}

extern "C" void kernel_launch(void* const* d_in, const int* in_sizes, int n_in, void* d_out, int out_size,
                              void* d_ws, size_t ws_size, hipStream_t stream) {
  fused_kernel<<<256, 512, 0, stream>>>(d_in[0], d_in[1], d_in[2], d_in[4], d_in[5], d_in[6], d_in[7], d_out, d_ws);
}
```

```cpp
#include <hip/hip_runtime.h>
#include <stdint.h>
#include <type_traits>
#pragma clang diagnostic ignored "-Wint-to-pointer-cast"

#define DEV __device__ __forceinline__
#define LAS __attribute__((address_space(3)))
typedef __attribute__((ext_vector_type(4))) short s16x4;
typedef __attribute__((ext_vector_type(8))) short s16x8;
typedef __attribute__((ext_vector_type(8))) __bf16 bf16x8;
typedef __attribute__((ext_vector_type(2))) __bf16 bf16x2;
typedef __attribute__((ext_vector_type(8))) _Float16 f16x8;
typedef __attribute__((ext_vector_type(2))) _Float16 f16x2;
typedef __attribute__((ext_vector_type(2))) float f32x2;
typedef __attribute__((ext_vector_type(4))) float f32x4;
typedef __attribute__((ext_vector_type(16))) float f32x16;
typedef __attribute__((ext_vector_type(2))) unsigned u32x2;
typedef __attribute__((ext_vector_type(4))) unsigned u32x4;
typedef __amdgpu_buffer_rsrc_t rsrc_t;

constexpr unsigned WS_E = 0;
constexpr unsigned WS_ZF = 4096;
constexpr unsigned WS_HF = WS_ZF + 224 * 128;
constexpr unsigned WS_AF = WS_HF + 224 * 128;
constexpr unsigned WS_CS = 65536;
constexpr unsigned WS_Z = 131072;
constexpr unsigned WS_A = WS_Z + 29 * 16 * 4096;
constexpr unsigned WS_H = WS_A + 32 * 2 * 29 * 1024;
constexpr unsigned WS_END = WS_H + 32 * 112 * 2048;
constexpr unsigned OOB = 0x80000000u;
constexpr int SPIN_CAP = 1 << 16;
#define CODE_BYTES 32768

constexpr unsigned L_C0 = 57344;
constexpr unsigned L_UH = 7 * 16384;
constexpr unsigned L_GATH = 8 * 16384;
constexpr unsigned L_STAT = 147456;
constexpr unsigned L_RROW = L_STAT + 4096;
constexpr unsigned L_DONE = L_RROW + 512;
DEV constexpr unsigned xblk(int kc) { return (unsigned)((0x8217034u >> (4 * kc)) & 15u) * 16384u; }

#define WAITBAR(n) asm volatile("s_waitcnt vmcnt(" #n ") lgkmcnt(0)\n\ts_barrier" ::: "memory")
#define LBAR() asm volatile("s_waitcnt lgkmcnt(0)\n\ts_barrier" ::: "memory")
#define PIN(x) asm volatile("" : "+v"(x))
#define SB() __builtin_amdgcn_sched_barrier(0)
#define STAMP(i)
#define STAMP_OUT()
#define WAITVM(n) asm volatile("s_waitcnt vmcnt(" #n ")" ::: "memory")

DEV u32x4 mk_srd(const void* p, unsigned bytes) {
  uint64_t a = (uint64_t)p;
  u32x4 r = {(unsigned)a, (unsigned)(a >> 32) & 0xffffu, bytes, 0x00020000u};
  return r;
}
DEV void dma16(unsigned lds, unsigned voff, u32x4 srd, unsigned soff) {
  asm volatile("s_mov_b32 m0, %0\n\ts_nop 0\n\tbuffer_load_dwordx4 %1, %2, %3 offen lds"
               :: "s"(lds), "v"(voff), "s"(srd), "s"(soff) : "memory");
}
DEV u32x4 lds128(unsigned a) { return *(LAS u32x4*)a; }
DEV void lds128w(unsigned a, u32x4 v) { *(LAS u32x4*)a = v; }
DEV u32x4 trfrag(unsigned a0, unsigned a1) {
  s16x4 lo = __builtin_amdgcn_ds_read_tr16_b64_v4i16((LAS s16x4*)a0);
  s16x4 hi = __builtin_amdgcn_ds_read_tr16_b64_v4i16((LAS s16x4*)a1);
  return __builtin_bit_cast(u32x4, __builtin_shufflevector(lo, hi, 0, 1, 2, 3, 4, 5, 6, 7));
}
DEV f32x16 mfma_bf(u32x4 a, u32x4 b, f32x16 c) {
  return __builtin_amdgcn_mfma_f32_32x32x16_bf16(__builtin_bit_cast(bf16x8, a), __builtin_bit_cast(bf16x8, b), c, 0, 0, 0);
}
DEV f32x16 mfma_h(u32x4 a, u32x4 b, f32x16 c) {
  return __builtin_amdgcn_mfma_f32_32x32x16_f16(__builtin_bit_cast(f16x8, a), __builtin_bit_cast(f16x8, b), c, 0, 0, 0);
}
DEV unsigned pk_bf(float lo, float hi) {
  f32x2 p = {lo, hi};
  return __builtin_bit_cast(unsigned, __builtin_convertvector(p, bf16x2));
}
DEV unsigned pk_h(float lo, float hi) {
  f32x2 p = {lo, hi};
  return __builtin_bit_cast(unsigned, __builtin_convertvector(p, f16x2));
}
DEV unsigned pk_bf2(f32x2 p) { return __builtin_bit_cast(unsigned, __builtin_convertvector(p, bf16x2)); }
DEV unsigned pk_h2(f32x2 p) { return __builtin_bit_cast(unsigned, __builtin_convertvector(p, f16x2)); }
DEV f32x2 pr(const f32x16& v, int r) { return f32x2{v[r], v[r + 1]}; }
DEV f32x2 fma2(f32x2 a, f32x2 b, f32x2 c) { return __builtin_elementwise_fma(a, b, c); }
DEV f32x2 bc(float a) { return f32x2{a, a}; }
DEV float bf_lo(unsigned v) { return __builtin_bit_cast(float, v << 16); }
DEV float bf_hi(unsigned v) { return __builtin_bit_cast(float, v & 0xffff0000u); }
DEV float bf16r(float x) { return bf_lo(pk_bf(x, 0.f)); }
DEV float dot2(unsigned a, unsigned b, float c) {
  return __builtin_amdgcn_fdot2_f32_bf16(__builtin_bit_cast(bf16x2, a), __builtin_bit_cast(bf16x2, b), c, false);
}
DEV float cosr(float rev) { return __builtin_amdgcn_cosf(rev); }
DEV float sinr(float rev) { return __builtin_amdgcn_sinf(rev); }
DEV float rev448(float n) {
  float q = __builtin_floorf(n * (1.0f / 448.0f));
  return __builtin_fmaf(q, -448.0f, n) * (1.0f / 448.0f);
}
DEV void poll(u32x4 srd, unsigned voff, u32x2 tag) {
#pragma unroll 1
  for (int n = 0; n < SPIN_CAP; ++n) {
    u32x2 v;
    asm volatile("buffer_load_dwordx2 %0, %1, %2, 0 offen sc1\n\ts_waitcnt vmcnt(0)"
                 : "=&v"(v) : "v"(voff), "s"(srd) : "memory");
    if (__builtin_amdgcn_ballot_w64(v[0] != tag[0] || v[1] != tag[1]) == 0) break;
    __builtin_amdgcn_s_sleep(1);
  }
}
DEV u32x2 mk_tag(u32x2 ev) {
  uint64_t e = ((uint64_t)__builtin_amdgcn_readfirstlane(ev[1]) << 32) | (unsigned)__builtin_amdgcn_readfirstlane(ev[0]);
  e = e * 6364136223846793005ull + 1442695040888963407ull;
  return u32x2{(unsigned)e, (unsigned)(e >> 32)};
}
template <int A, int B, class Fn> DEV void sfor(Fn&& f) {
  if constexpr (A < B) { f(std::integral_constant<int, A>{}); sfor<A + 1, B>(f); }
}
DEV float xor_add(float v, int m) { return v + __shfl_xor(v, m); }

__global__ void __launch_bounds__(512, 2)
fused_kernel(const void* __restrict__ px, const void* __restrict__ pwr, const void* __restrict__ pwi,
             const void* __restrict__ pw1, const void* __restrict__ pb1, const void* __restrict__ pw2,
             const void* __restrict__ pb2, void* __restrict__ pout, void* __restrict__ pws) {
  __shared__ __attribute__((aligned(1024))) char smem[163840];
  const unsigned LB = (unsigned)(uintptr_t)(LAS char*)smem;
  const int tid = threadIdx.x;
  const int lane = tid & 63;
  const int w = __builtin_amdgcn_readfirstlane(tid >> 6);
  const int l31 = lane & 31, h = lane >> 5;
  const int xcd = blockIdx.x & 7, loc = blockIdx.x >> 3;

  const u32x4 sws = mk_srd(pws, WS_END);
  const rsrc_t rws = __builtin_amdgcn_make_buffer_rsrc(pws, 0, WS_END, 0x00020000);
  STAMP(0);
  unsigned pfv = 0;
  if (w >= 4) {
    const u32x4 sc = mk_srd((const void*)__builtin_amdgcn_s_getpc(), CODE_BYTES);
    asm volatile("buffer_load_dword %0, %1, %2, 0 offen" : "=v"(pfv) : "v"((tid - 256) * 128), "s"(sc) : "memory");
  }
  u32x2 ev;
  asm volatile("s_nop 4\n\tbuffer_load_dwordx2 %0, off, %1, 0 sc1" : "=v"(ev) : "s"(sws) : "memory");

  if (loc >= 28) {
    const int pq = xcd, qt = loc - 28;
    const int ch = tid & 7, rw = tid >> 3;
    const rsrc_t rw1 = __builtin_amdgcn_make_buffer_rsrc((void*)pw1, 0, 448 * 1792 * 2, 0x00020000);
    const rsrc_t rb1 = __builtin_amdgcn_make_buffer_rsrc((void*)pb1, 0, 1792 * 2, 0x00020000);
    const rsrc_t rwr = __builtin_amdgcn_make_buffer_rsrc((void*)pwr, 0, 2048 * 225 * 2, 0x00020000);
    const rsrc_t rwi = __builtin_amdgcn_make_buffer_rsrc((void*)pwi, 0, 2048 * 225 * 2, 0x00020000);
    const int col0 = 896 * (xcd & 1) + 224 * (xcd >> 1) + 56 * qt;
    u32x4 cw[7];
    {
      const unsigned o = (ch < 7) ? (unsigned)(rw * 3584 + col0 * 2 + ch * 16) : OOB;
#pragma unroll
      for (int i = 0; i < 7; ++i) cw[i] = __builtin_amdgcn_raw_buffer_load_b128(rw1, o, i * 64 * 3584, 0);
    }
    const unsigned b1v = __builtin_amdgcn_raw_buffer_load_b16(rb1, tid < 56 ? (unsigned)(col0 + tid) * 2u : OOB, 0, 0);
    u32x4 wv[8];
    unsigned wx[8];
#pragma unroll
    for (int i = 0; i < 4; ++i) {
      const int idx = rw + 64 * (i & 1), p = 4 * pq + (idx & 3), k2 = idx >> 2;
      const int k = (i >> 1) ? (p ? ((2048 - p + 64 * k2) & 2047) : (32 + 64 * k2)) : (p + 64 * k2);
      const unsigned o = (unsigned)(k * 225 + 56 * qt + 8 * ch) * 2u;
      const unsigned o4 = (ch < 7) ? o : OOB, o1 = (ch == 7 && qt == 3) ? o : OOB;
      wv[i] = __builtin_amdgcn_raw_buffer_load_b128(rwr, o4, 0, 0);
      wv[4 + i] = __builtin_amdgcn_raw_buffer_load_b128(rwi, o4, 0, 0);
      wx[i] = __builtin_amdgcn_raw_buffer_load_b16(rwr, o1, 0, 0);
      wx[4 + i] = __builtin_amdgcn_raw_buffer_load_b16(rwi, o1, 0, 0);
    }
    SB();
    {
      f32x2 cs[4] = {};
#pragma unroll
      for (int i = 0; i < 7; ++i)
#pragma unroll
        for (int e = 0; e < 4; ++e) cs[e] += f32x2{bf_lo(cw[i][e]), bf_hi(cw[i][e])};
      float c8[8];
#pragma unroll
      for (int e = 0; e < 8; ++e) c8[e] = xor_add(xor_add(xor_add(cs[e >> 1][e & 1], 8), 16), 32);
      if (lane < 8) {
        *(LAS f32x4*)(LB + 81920u + (unsigned)(w * 64 + lane * 8) * 4u) = f32x4{c8[0], c8[1], c8[2], c8[3]};
        *(LAS f32x4*)(LB + 81920u + (unsigned)(w * 64 + lane * 8) * 4u + 16u) = f32x4{c8[4], c8[5], c8[6], c8[7]};
      }
    }
    LBAR();
    asm volatile("" : "+v"(ev), "+v"(pfv));
    const u32x2 tag = mk_tag(ev);
    if (w == 0) {
      float cs = 0.f;
#pragma unroll
      for (int k = 0; k < 8; ++k) cs += *(LAS float*)(LB + 81920u + (unsigned)(k * 64 + lane) * 4u);
      const float hi = bf16r(cs);
      const u32x4 gr = {pk_bf(hi, cs - hi), tag[0], b1v, tag[1]};
      __builtin_amdgcn_raw_buffer_store_b128(gr, rws, lane < 56 ? WS_CS + (unsigned)(col0 + lane) * 16u : OOB, 0, 16);
    }
    STAMP(1);
#pragma unroll
    for (int i = 0; i < 8; ++i) {
      wv[i][0] |= wx[i];
      lds128w(LB + (unsigned)(((i >> 1) * 128 + rw + 64 * (i & 1)) * 144 + ch * 16), wv[i]);
    }
    const bool active = (w < 7) || (qt == 3);
    const int g = 7 * qt + w;
    const int kdl = lane & 7, pl = (lane >> 3) & 3;
    const int p = 4 * pq + pl, kd = 8 * g + kdl;
    u32x4 FR[4], FI[4], IR[4], II[4];
#pragma unroll
    for (int t = 0; t < 4; ++t) {
      float c[4], sn[4];
#pragma unroll
      for (int e = 0; e < 4; ++e) {
        const float a = (float)(l31 * (8 * t + 4 * h + e)) * (1.0f / 32.0f);
        c[e] = cosr(a);
        sn[e] = sinr(a);
        FR[t][e] = pk_h(c[e], sn[e]);
        FI[t][e] = pk_h(-sn[e], c[e]);
      }
      IR[t] = u32x4{pk_bf(c[0], c[1]), pk_bf(c[2], c[3]), pk_bf(-sn[0], -sn[1]), pk_bf(-sn[2], -sn[3])};
      II[t] = u32x4{pk_bf(sn[0], sn[1]), pk_bf(sn[2], sn[3]), pk_bf(c[0], c[1]), pk_bf(c[2], c[3])};
      PIN(FR[t]); PIN(FI[t]); PIN(IR[t]); PIN(II[t]);
    }
    LBAR();
    if (active) {
      f32x2 wpr[8], wpi[8], wmr[8], wmi[8];
      const float wsc = ((kd == 0 || kd == 224) ? 1.0f : 2.0f) / (2048.0f * 448.0f);
      const unsigned wa = LB + (unsigned)((16 * h + pl) * 144 + (8 * w + kdl) * 2);
#pragma unroll
      for (int r = 0; r < 16; ++r) {
        const unsigned o = (unsigned)(((r & 3) + 8 * (r >> 2)) * 4 * 144);
        wpr[r >> 1][r & 1] = bf_lo(*(LAS unsigned short*)(wa + o));
        wmr[r >> 1][r & 1] = bf_lo(*(LAS unsigned short*)(wa + o + 128 * 144));
        wpi[r >> 1][r & 1] = bf_lo(*(LAS unsigned short*)(wa + o + 256 * 144));
        wmi[r >> 1][r & 1] = bf_lo(*(LAS unsigned short*)(wa + o + 384 * 144));
      }
      float ff = pq ? 1.0f : 0.0f;
      float cph = pq ? 1.0f : cosr((float)l31 * (1.0f / 64.0f));
      float sph = pq ? 0.0f : sinr((float)l31 * (1.0f / 64.0f));
      PIN(ff); PIN(cph); PIN(sph);
#pragma unroll
      for (int r = 0; r < 8; ++r) {
        wpr[r] *= bc(wsc); wpi[r] *= bc(wsc); wmr[r] *= bc(wsc); wmi[r] *= bc(wsc);
        PIN(wpr[r]); PIN(wpi[r]); PIN(wmr[r]); PIN(wmi[r]);
      }
      unsigned zo = (kd > 224) ? OOB : WS_Z + (unsigned)((g * 16 + 2 * h) * 4096 + p * 128 + kdl * 16);
      unsigned ao = WS_A + (unsigned)((((lane >> 4) * 2 + (pq >> 2)) * 29 + g) * 1024 + (pq & 3) * 256 + (lane & 15) * 16);
      unsigned tw = LB + 90112u + (unsigned)(w * 8192 + l31 * 256);
      unsigned tr = LB + 90112u + (unsigned)(w * 8192 + (lane >> 4) * 256);
      PIN(zo); PIN(ao); PIN(tw); PIN(tr);
      STAMP(2);
      const int jz = (g == 28) ? 0 : (g >> 1);
      poll(sws, WS_ZF + (unsigned)(((lane & 15) * 14 + jz) * 128 + (pq >> 1) * 8), tag);
      STAMP(3);

      u32x4 z[8];
#pragma unroll
      for (int i = 0; i < 8; ++i)
        z[i] = __builtin_amdgcn_raw_buffer_load_b128(rws, zo, (4 * (i >> 1) + (i & 1)) * 4096, 16);
      SB();
      f32x16 xpr = {}, xpi = {}, xmr = {}, xmi = {};
#pragma unroll
      for (int t = 0; t < 4; ++t) {
        const u32x4 zp = {z[2 * t][0], z[2 * t][1], z[2 * t + 1][0], z[2 * t + 1][1]};
        xpr = mfma_h(FR[t], zp, xpr);
        xpi = mfma_h(FI[t], zp, xpi);
      }
#pragma unroll
      for (int t = 0; t < 4; ++t) {
        const u32x4 zm = {z[2 * t][2], z[2 * t][3], z[2 * t + 1][2], z[2 * t + 1][3]};
        xmr = mfma_h(FR[t], zm, xmr);
        xmi = mfma_h(FI[t], zm, xmi);
      }
      STAMP(4);
      f32x16 ypr = {}, ypi = {}, ymr = {}, ymi = {};
#pragma unroll
      for (int t = 0; t < 4; ++t) {
        u32x4 pp;
#pragma unroll
        for (int e = 0; e < 2; ++e) {
          const int r = 4 * t + 2 * e, q = 2 * t + e;
          pp[e] = pk_bf2(fma2(pr(xpr, r), wpr[q], -(pr(xpi, r) * wpi[q])));
          pp[2 + e] = pk_bf2(fma2(pr(xpr, r), wpi[q], pr(xpi, r) * wpr[q]));
        }
        ypr = mfma_bf(pp, IR[t], ypr);
        ypi = mfma_bf(pp, II[t], ypi);
      }
#pragma unroll
      for (int t = 0; t < 4; ++t) {
        u32x4 pm;
#pragma unroll
        for (int e = 0; e < 2; ++e) {
          const int r = 4 * t + 2 * e, q = 2 * t + e;
          pm[e] = pk_bf2(fma2(pr(xmr, r), wmr[q], -(pr(xmi, r) * wmi[q])));
          pm[2 + e] = pk_bf2(fma2(pr(xmr, r), wmi[q], pr(xmi, r) * wmr[q]));
        }
        ymr = mfma_bf(pm, IR[t], ymr);
        ymi = mfma_bf(pm, II[t], ymi);
      }
      STAMP(5);
#pragma unroll
      for (int q = 0; q < 4; ++q) {
        u32x4 uc, us;
#pragma unroll
        for (int e = 0; e < 2; ++e) {
          const int r = 4 * q + 2 * e;
          f32x2 acr, aci, asr, asi;
          if (q == 0) {
            acr = fma2(bc(ff), pr(ymr, r), pr(ypr, r));
            aci = fma2(bc(ff), pr(ymi, r), pr(ypi, r));
            asr = fma2(bc(cph), pr(ymi, r), fma2(bc(sph), pr(ymr, r), -(bc(ff) * pr(ypi, r))));
            asi = fma2(bc(sph), pr(ymi, r), fma2(bc(ff), pr(ypr, r), -(bc(cph) * pr(ymr, r))));
          } else {
            acr = pr(ypr, r) + pr(ymr, r);
            aci = pr(ypi, r) + pr(ymi, r);
            asr = pr(ymi, r) - pr(ypi, r);
            asi = pr(ypr, r) - pr(ymr, r);
          }
          uc[e] = pk_bf2(acr); uc[2 + e] = pk_bf2(aci);
          us[e] = pk_bf2(asr); us[2 + e] = pk_bf2(asi);
        }
        lds128w(tw + (unsigned)(((8 * h + 2 * q) ^ (l31 & 7)) * 16), uc);
        lds128w(tw + (unsigned)(((8 * h + 2 * q + 1) ^ (l31 & 7)) * 16), us);
      }
#pragma unroll
      for (int i = 0; i < 8; ++i) {
        const u32x4 v = lds128(tr + (unsigned)(i * 1024 + (((lane & 15) ^ ((4 * i + (lane >> 4)) & 7)) * 16)));
        __builtin_amdgcn_raw_buffer_store_b128(v, rws, ao + i * 8 * 29 * 1024, 0, 16);
      }
      STAMP(6);
      WAITVM(0);
      if (lane == 0) __builtin_amdgcn_raw_buffer_store_b64(tag, rws, WS_AF + (unsigned)((pq * 4 + qt) * 128 + w * 8), 0, 16);
      STAMP(7);
    }
    STAMP_OUT();
    return;
  }

  const int rg = 4 * (xcd >> 1) + loc / 7, j = 7 * (xcd & 1) + loc % 7;
  const int s2 = 4 * xcd + loc / 7, cg = loc % 7;
  const int rgw = w & 3, grp = w >> 2;
  const u32x4 sx = mk_srd(px, 2048 * 448 * 2), sw1 = mk_srd(pw1, 448 * 1792 * 2), sw2 = mk_srd(pw2, 448 * 1792 * 2);
  const unsigned vx = (unsigned)((32 * (8 * rgw + (lane >> 3)) + 2 * rg) * 896 + (((lane & 7) ^ ((4 * rgw + (lane >> 4)) & 7)) * 16));
  auto xdma = [&](int kc) {
#pragma unroll
    for (int q = 0; q < 4; ++q)
      dma16(LB + xblk(kc) + q * 4096 + rgw * 1024, vx, sx, kc * 128 + (q & 1) * 32 * 32 * 896 + (q >> 1) * 896);
  };
  const unsigned swz16 = (unsigned)(((lane & 7) ^ ((lane >> 2) & 4)) * 16);
  const unsigned vw1 = (unsigned)(lane >> 3) * 3584u + swz16;
  auto w1dma = [&](int c, int q) {
    dma16(LB + (c ? 0u : L_C0) + q * 1024, vw1, sw1, q * 8 * 3584 + 256 * j + 128 * c);
  };
  if (grp == 0) { xdma(0); xdma(1); xdma(2); xdma(3); }
  if (tid < 8) *(LAS unsigned*)(LB + L_DONE + tid * 4) = 0u;
  STAMP(1);

  u32x4 F[14];
  {
    const bool sp = (j == 0) && (l31 == 16);
    const float kdf = sp ? 224.0f : (float)(16 * j + (l31 & 15));
    const float ph = ((l31 >> 4) && !sp) ? 0.25f : 0.0f;
    const float tw = 2.0f * cosr(rev448(32.0f * kdf));
    const float d0 = (float)(16 * grp + 8 * h), dl = kdf * (1.0f / 448.0f);
    const float a0 = rev448(d0 * kdf) + ph, a1 = rev448((d0 + 32.0f) * kdf) + ph;
    f32x2 f0[4], f1[4];
#pragma unroll
    for (int e = 0; e < 8; ++e) {
      f0[e >> 1][e & 1] = cosr(__builtin_fmaf((float)e, dl, a0));
      f1[e >> 1][e & 1] = cosr(__builtin_fmaf((float)e, dl, a1));
    }
#pragma unroll
    for (int i = 0; i < 14; ++i) {
#pragma unroll
      for (int e = 0; e < 4; ++e) {
        F[i][e] = pk_bf2(f0[e]);
        const f32x2 n = fma2(bc(tw), f1[e], -f0[e]);
        f0[e] = f1[e];
        f1[e] = n;
      }
      PIN(F[i]);
    }
  }
  u32x4 GA[2][2] = {};
  float cph[2] = {}, sph[2] = {};
  if (grp == 1) {
    const int hh = (l31 >> 2) & 1, reg = (l31 & 3) + 4 * (l31 >> 3);
    const int s2a = 2 * rg + (rgw >> 1);
#pragma unroll
    for (int mt = 0; mt < 2; ++mt) {
      const int p = 16 * mt + 8 * hh + (reg >> 1), cs = reg & 1;
      const bool sp = (p == 0) && cs;
      const float ca = sp ? 0.5f : (float)p * (1.0f / 64.0f);
      const float cb = __builtin_fmaf(ca, (float)(32 * (rgw & 1) + 4 * h), sp ? 0.0f : (float)(p * s2a) * (1.0f / 2048.0f) - 0.25f * (float)cs);
      const float tw = 2.0f * cosr(8.0f * ca);
      f32x2 g0[2], g1[2];
#pragma unroll
      for (int e = 0; e < 4; ++e) {
        g0[e >> 1][e & 1] = cosr(__builtin_fmaf(ca, (float)e, cb));
        g1[e >> 1][e & 1] = cosr(__builtin_fmaf(ca, (float)(e + 8), cb));
      }
#pragma unroll
      for (int e = 0; e < 2; ++e) {
        const f32x2 g2 = fma2(bc(tw), g1[e], -g0[e]), g3 = fma2(bc(tw), g2, -g1[e]);
        GA[mt][0][e] = pk_h2(g0[e]); GA[mt][0][2 + e] = pk_h2(g1[e]);
        GA[mt][1][e] = pk_h2(g2); GA[mt][1][2 + e] = pk_h2(g3);
      }
      PIN(GA[mt][0]); PIN(GA[mt][1]);
    }
#pragma unroll
    for (int sg = 0; sg < 2; ++sg) {
      const float ang = ((tid & 255) >> 3) ? 0.0f : (float)(2 * rg + sg) * (1.0f / 64.0f);
      cph[sg] = cosr(ang); sph[sg] = sinr(ang);
      PIN(cph[sg]); PIN(sph[sg]);
    }
    xdma(4); xdma(5); xdma(6);
#pragma unroll
    for (int q = 0; q < 8; ++q) w1dma(0, 24 + 4 * q + rgw);
  }
  STAMP(2);

  u32x4 X[28];
  f32x16 U = {};
  float sum0 = 0.f, sum1 = 0.f, sq0 = 0.f, sq1 = 0.f;
  const unsigned xr = LB + (unsigned)(32 * rgw + l31) * 128u;
  const unsigned xs = (unsigned)((2 * grp + h) ^ ((l31 >> 1) & 7));
  auto xread = [&](int u) { X[u] = lds128(xr + ((xs ^ (2 * (u & 3))) * 16) + xblk(u >> 2)); };
  auto xstat = [&](int u) {
    sum0 = dot2(X[u][0], 0x3f803f80u, sum0); sq0 = dot2(X[u][0], X[u][0], sq0);
    sum1 = dot2(X[u][1], 0x3f803f80u, sum1); sq1 = dot2(X[u][1], X[u][1], sq1);
    sum0 = dot2(X[u][2], 0x3f803f80u, sum0); sq0 = dot2(X[u][2], X[u][2], sq0);
    sum1 = dot2(X[u][3], 0x3f803f80u, sum1); sq1 = dot2(X[u][3], X[u][3], sq1);
    PIN(sum0); PIN(sum1); PIN(sq0); PIN(sq1);
  };
  if (grp == 0) WAITVM(8);
  LBAR();
  STAMP(3);
#pragma unroll
  for (int u = 0; u < 8; u += 2) xread(u);
#pragma unroll
  for (int u = 1; u < 8; u += 2) xread(u);
  SB();
#pragma unroll
  for (int u = 0; u < 8; u += 2) { U = mfma_bf(X[u], F[u >> 1], U); xstat(u); SB(); }
  if (grp == 0) WAITVM(0);
  LBAR();
  STAMP(4);
#pragma unroll
  for (int u = 8; u < 16; u += 2) xread(u);
#pragma unroll
  for (int u = 9; u < 16; u += 2) xread(u);
  if (grp == 0) {
#pragma unroll
    for (int q = 0; q < 6; ++q) w1dma(0, 4 * q + rgw);
  } else {
    w1dma(1, 48 + rgw); w1dma(1, 52 + rgw);
  }
  SB();
#pragma unroll
  for (int u = 8; u < 16; u += 2) { U = mfma_bf(X[u], F[u >> 1], U); xstat(u); SB(); }
  if (grp == 1) WAITVM(10);
  LBAR();
  STAMP(5);
#pragma unroll
  for (int u = 16; u < 28; u += 2) xread(u);
#pragma unroll
  for (int u = 17; u < 28; u += 2) xread(u);
  w1dma(1, w); w1dma(1, 8 + w);
  SB();
#pragma unroll
  for (int u = 16; u < 28; u += 2) { U = mfma_bf(X[u], F[u >> 1], U); xstat(u); SB(); }
  *(LAS f32x2*)(LB + L_STAT + (unsigned)((32 * rgw + l31) * 4 + 2 * grp + h) * 8u) = f32x2{sum0 + sum1, sq0 + sq1};
  if (grp == 0) {
#pragma unroll
    for (int q = 0; q < 4; ++q)
      *(LAS f32x4*)(LB + L_UH + (unsigned)(rgw * 4096 + q * 1024 + lane * 16)) = f32x4{U[4 * q], U[4 * q + 1], U[4 * q + 2], U[4 * q + 3]};
  } else {
    WAITVM(4);
  }
  STAMP(6);
  LBAR();
  STAMP(7);
  u32x4 cg0, cg1;
  const unsigned cso = WS_CS + (unsigned)(128 * j + 64 * grp + l31) * 16u;
  asm volatile("buffer_load_dwordx4 %0, %2, %3, 0 offen sc1\n\tbuffer_load_dwordx4 %1, %2, %3, 0 offen offset:512 sc1"
               : "=&v"(cg0), "=&v"(cg1) : "v"(cso), "s"(sws) : "memory");
  asm volatile("" : "+v"(ev), "+v"(pfv));
  const u32x2 tag = mk_tag(ev);
  float sig, rstd;
  u32x4 xaug;
  {
    const f32x4 a = *(LAS f32x4*)(LB + L_STAT + (unsigned)(32 * rgw + l31) * 32u);
    const f32x4 b = *(LAS f32x4*)(LB + L_STAT + (unsigned)(32 * rgw + l31) * 32u + 16u);
    const f32x2 t = (f32x2{a[0], a[1]} + f32x2{a[2], a[3]}) + (f32x2{b[0], b[1]} + f32x2{b[2], b[3]});
    const float mean = t[0] * (1.0f / 448.0f);
    const float var = __builtin_fmaxf(__builtin_fmaf(-mean, mean, t[1] * (1.0f / 448.0f)), 0.0f);
    sig = bf16r(__builtin_amdgcn_sqrtf(bf16r(bf16r(var) + 1e-5f)));
    rstd = __builtin_amdgcn_rcpf(sig);
    xaug = u32x4{pk_bf(-mean, -mean), pk_bf(sig, 0.f), 0u, 0u};
  }
  if (grp == 1) {
    __builtin_amdgcn_s_setprio(2);
    *(LAS float*)(LB + L_RROW + (unsigned)(rgw * 32 + l31) * 4u) = rstd;
    const u32x4 faug = {(j == 0 && lane == 0) ? 0x43e0u : 0u, 0u, 0u, 0u};
    U = mfma_bf(xaug, faug, U);
    u32x4 ub[2];
#pragma unroll
    for (int q = 0; q < 4; ++q) {
      const f32x4 uo = *(LAS f32x4*)(LB + L_UH + (unsigned)(rgw * 4096 + q * 1024 + lane * 16));
      const f32x4 rr = *(LAS f32x4*)(LB + L_RROW + (unsigned)(rgw * 32 + 8 * q + 4 * h) * 4u);
      ub[q >> 1][2 * (q & 1)] = pk_h2((pr(U, 4 * q) + f32x2{uo[0], uo[1]}) * f32x2{rr[0], rr[1]});
      ub[q >> 1][2 * (q & 1) + 1] = pk_h2((pr(U, 4 * q + 2) + f32x2{uo[2], uo[3]}) * f32x2{rr[2], rr[3]});
    }
#pragma unroll
    for (int mt = 0; mt < 2; ++mt) {
      f32x16 za = {};
      za = mfma_h(GA[mt][0], ub[0], za);
      za = mfma_h(GA[mt][1], ub[1], za);
#pragma unroll
      for (int q = 0; q < 2; ++q) {
        u32x4 v;
#pragma unroll
        for (int e = 0; e < 4; ++e) v[e] = pk_h2(pr(za, 8 * q + 2 * e));
        lds128w(LB + L_GATH + (unsigned)(rgw * 4096 + l31 * 128 + (((4 * mt + 2 * h + q) ^ (l31 & 7)) * 16)), v);
      }
    }
    STAMP(8);
    LBAR();
    STAMP(9);
    const int p = (tid & 255) >> 3, kdl = tid & 7;
    const unsigned ga = LB + L_GATH + (unsigned)(kdl * 128 + (((p >> 2) ^ kdl) * 16) + (p & 3) * 4);
    const float ff = p ? 1.0f : 0.0f;
    u32x4 zx;
#pragma unroll
    for (int gl = 0; gl < 2; ++gl) {
      const bool sp = (gl == 0) && (j == 0) && (kdl == 0);
      u32x4 z;
#pragma unroll
      for (int sg = 0; sg < 2; ++sg) {
        float v[4];
#pragma unroll
        for (int uv = 0; uv < 2; ++uv) {
          const f16x2 ts = *(LAS f16x2*)(ga + (unsigned)(2 * sg * 4096 + uv * 2048 + gl * 1024)) +
                           *(LAS f16x2*)(ga + (unsigned)((2 * sg + 1) * 4096 + uv * 2048 + gl * 1024));
          v[2 * uv] = (float)ts[0];
          v[2 * uv + 1] = (float)ts[1];
        }
        const float a = v[0], e = v[1], c = sp ? 0.f : v[2], b = sp ? 0.f : v[3];
        float o[4] = {a + ff * b, c - ff * e, ff * a - (cph[sg] * b - sph[sg] * e), ff * c + (cph[sg] * e + sph[sg] * b)};
#pragma unroll
        for (int k = 0; k < 4; ++k) asm("" : "+v"(o[k]));
        z[sg] = pk_h(o[0], o[1]);
        z[2 + sg] = pk_h(o[2], o[3]);
        if (gl == 0) {
          float q[3] = {-ff * v[3], ff * v[2] + sph[sg] * v[3], cph[sg] * v[3]};
#pragma unroll
          for (int k = 0; k < 3; ++k) asm("" : "+v"(q[k]));
          zx[sg] = pk_h(v[2], q[0]);
          zx[2 + sg] = pk_h(q[1], q[2]);
        }
      }
      __builtin_amdgcn_raw_buffer_store_b128(z, rws, WS_Z + (unsigned)(((2 * j + gl) * 16 + rg) * 4096 + (tid & 255) * 16), 0, 16);
    }
    __builtin_amdgcn_raw_buffer_store_b128(zx, rws, (j == 0 && kdl == 0) ? WS_Z + (unsigned)((28 * 16 + rg) * 4096 + p * 128) : OOB, 0, 16);
    __builtin_amdgcn_s_setprio(0);
    STAMP(10);
    WAITBAR(3);
  }
  STAMP(11);

  const int tq = (lane >> 2) & 3, tp = lane & 3, tg = (lane >> 4) & 1;
  const unsigned w1b = LB + (grp ? 0u : L_C0) + (unsigned)((8 * h + tq) * 128 + 64 * (tq >> 1) + 32 * tg + 8 * tp);
  const unsigned w1e = w1b + 2048u * grp, w1o = w1b + 2048u * (1 - grp);
  auto w1frag = [&](int c, int u) {
    const unsigned a = (((u & 1) ? w1o : w1e) ^ (64u * c)) + 2048u * (u & ~1);
    return trfrag(a, a + 512);
  };
  const float gc1 = -2.302208198f * rstd, gc2 = gc1 * 0.044715f * rstd * rstd;
  const unsigned ho = WS_H + (unsigned)((((2 * rg + (rgw >> 1)) * 112 + 56 * grp + 4 * j) * 2 + (rgw & 1)) * 1024 + lane * 16);
  auto gelu2 = [&](f32x2 u) {
    const f32x2 ar = fma2(u * u, bc(gc2), bc(gc1)) * u;
    const f32x2 ex = {__builtin_amdgcn_exp2f(ar[0]), __builtin_amdgcn_exp2f(ar[1])};
    const f32x2 dn = fma2(ex, bc(sig), bc(sig));
    return pk_bf2(u * f32x2{__builtin_amdgcn_rcpf(dn[0]), __builtin_amdgcn_rcpf(dn[1])});
  };
  f32x16 h0 = {}, h1 = {};
#define SEQ_C(i) ((i) < 16 ? 0 : (i) < 24 ? 1 : (i) < 36 ? 0 : 1)
#define SEQ_U(i) ((i) < 16 ? 12 + (i) : (i) < 24 ? (i) - 4 : (i) < 36 ? (i) - 24 : (i) < 44 ? (i) - 16 : (i) - 44)
  {
    u32x4 wf[3];
    wf[0] = w1frag(SEQ_C(0), SEQ_U(0));
    wf[1] = w1frag(SEQ_C(1), SEQ_U(1));
    u32x4 ho0[2];
sfor<0, 56>([&](auto ic) {
      constexpr int i = ic;
      if (i == 22 && grp == 0) { STAMP(8); WAITBAR(12); STAMP(9); }
      if (i == 8 && grp == 1) {
        WAITVM(0);
        if (lane == 0) __builtin_amdgcn_raw_buffer_store_b64(tag, rws, WS_ZF + (unsigned)((rg * 14 + j) * 128 + rgw * 8), 0, 16);
        STAMP(12);
      }
      if constexpr (i + 2 < 56) wf[(i + 2) % 3] = w1frag(SEQ_C(i + 2), SEQ_U(i + 2));
      if (SEQ_C(i) == 0) h0 = mfma_bf(wf[i % 3], X[SEQ_U(i)], h0);
      else h1 = mfma_bf(wf[i % 3], X[SEQ_U(i)], h1);
      if (i < 16 && (i & 3) == 1 && grp == 0) { w1dma(1, 16 + 2 * (i & 12) + rgw); w1dma(1, 20 + 2 * (i & 12) + rgw); }
      if (i == 34) {
        WAITVM(8);
        asm volatile("" : "+v"(cg0), "+v"(cg1));
#pragma unroll 1
        for (int n = 0; n < SPIN_CAP; ++n) {
          if (__builtin_amdgcn_ballot_w64(cg0[1] != tag[0] || cg0[3] != tag[1] || cg1[1] != tag[0] || cg1[3] != tag[1]) == 0) break;
          asm volatile("buffer_load_dwordx4 %0, %2, %3, 0 offen sc1\n\tbuffer_load_dwordx4 %1, %2, %3, 0 offen offset:512 sc1\n\ts_waitcnt vmcnt(0)"
                       : "=&v"(cg0), "=&v"(cg1) : "v"(cso), "s"(sws) : "memory");
        }
        cg0 = u32x4{h ? 0u : cg0[0], h ? 0u : cg0[2], 0u, 0u};
        cg1 = u32x4{h ? 0u : cg1[0], h ? 0u : cg1[2], 0u, 0u};
      }
      if (i == 35) h0 = mfma_bf(cg0, xaug, h0);
      if (i == 36) h1 = mfma_bf(cg1, xaug, h1);
      if constexpr (i >= 38 && i < 54 && !(i & 1)) {
        constexpr int e = (i - 38) >> 1;
        ho0[e >> 2][e & 3] = gelu2(pr(h0, 2 * e));
        if ((e & 3) == 3) __builtin_amdgcn_raw_buffer_store_b128(ho0[e >> 2], rws, ho + (e >> 2) * 2048, 0, 16);
      }
      if (i < 38 || (i & 1)) SB();
    });
  }
  STAMP(13);
  const unsigned vw2 = (unsigned)(lane >> 3) * 896u + swz16;
  auto w2dma = [&](unsigned lds, int pc) {
    const int ks = 14 * rgw + (pc >> 1);
    dma16(lds, vw2, sw2, (128 * (ks >> 2) + 64 * grp + 16 * (ks & 3) + 8 * (pc & 1)) * 896 + 128 * cg);
  };
  const unsigned LR = LB + L_C0 + rgw * 22528, LQ = LB + rgw * 14336;
  if (grp == 0) {
    WAITBAR(2);
#pragma unroll
    for (int pc = 0; pc < 8; ++pc) w2dma(LR + pc * 1024, pc);
  } else {
    asm volatile("s_waitcnt lgkmcnt(0)" ::: "memory");
    if (lane == 0) *(LAS unsigned*)(LB + L_DONE + w * 4) = 1u;
  }
  SB();
  STAMP(14);
#pragma unroll
  for (int t = 0; t < 2; ++t) {
    u32x4 o;
#pragma unroll
    for (int e = 0; e < 4; ++e) o[e] = gelu2(pr(h1, 8 * t + 2 * e));
    __builtin_amdgcn_raw_buffer_store_b128(o, rws, ho + 4096 + t * 2048, 0, 16);
  }
  SB();
  STAMP(15);

  const rsrc_t rx = __builtin_amdgcn_make_buffer_rsrc((void*)px, 0, 2048 * 448 * 2, 0x00020000);
  const rsrc_t rb2 = __builtin_amdgcn_make_buffer_rsrc((void*)pb2, 0, 448 * 2, 0x00020000);
  const rsrc_t rout = __builtin_amdgcn_make_buffer_rsrc(pout, 0, 2048 * 448 * 2, 0x00020000);
  const unsigned hfo = WS_HF + (unsigned)((rg * 14 + j) * 128 + w * 8);
  const unsigned hpo = WS_HF + (unsigned)(((s2 >> 1) * 14 + min((14 * rgw + 4 * ((lane >> 1) & 3)) >> 2, (14 * rgw + 13) >> 2)) * 128 +
                                          (4 * grp + 2 * (s2 & 1) + (lane & 1)) * 8);
  const unsigned hi = WS_H + (unsigned)((s2 * 112 + 56 * grp + 14 * rgw) * 2048 + lane * 16);
  const unsigned w2l = (unsigned)((4 * h + tq) * 128 + 64 * (tq >> 1) + 32 * tg + 8 * tp);
  const unsigned oo = (unsigned)((32 * (tid >> 3) + s2) * 896 + 128 * cg + (tid & 7) * 16);
  u32x4 xres, b2v;
  f32x16 acc[2][2] = {};
  if (grp == 0) {
#pragma unroll
    for (int pc = 8; pc < 22; ++pc) w2dma(LR + pc * 1024, pc);
    WAITVM(14);
    if (lane == 0) __builtin_amdgcn_raw_buffer_store_b64(tag, rws, hfo, 0, 16);
    STAMP(16);
    WAITVM(1);
    u32x4 W2R[11][2];
#pragma unroll
    for (int t = 0; t < 11; ++t)
#pragma unroll
      for (int x = 0; x < 2; ++x) {
        const unsigned a = LR + (w2l ^ (64u * x)) + t * 2048;
        W2R[t][x] = trfrag(a, a + 1024);
      }
    asm volatile("s_waitcnt lgkmcnt(0)" ::: "memory");
    if (lane == 0) *(LAS unsigned*)(LB + L_DONE + w * 4) = 1u;
#pragma unroll
    for (int pc = 22; pc < 28; ++pc) w2dma(LR + (pc - 22) * 1024, pc);
    STAMP(17);
    poll(sws, hpo, tag);
    STAMP(18);
    u32x4 H[14][2];
#pragma unroll
    for (int t = 0; t < 10; ++t)
#pragma unroll
      for (int sh = 0; sh < 2; ++sh) H[t][sh] = __builtin_amdgcn_raw_buffer_load_b128(rws, hi + sh * 1024, t * 2048, 16);
    SB();
#pragma unroll
    for (int t = 0; t < 14; ++t) {
#pragma unroll
      for (int x = 0; x < 2; ++x) {
        u32x4 wf;
        if (t < 11) wf = W2R[t][x];
        else {
          const unsigned a = LR + (w2l ^ (64u * x)) + (t - 11) * 2048;
          wf = trfrag(a, a + 1024);
        }
        acc[x][0] = mfma_bf(wf, H[t][0], acc[x][0]);
        acc[x][1] = mfma_bf(wf, H[t][1], acc[x][1]);
      }
      if (t < 2) {
#pragma unroll
        for (int k = 0; k < 4; ++k)
          H[10 + 2 * t + (k >> 1)][k & 1] = __builtin_amdgcn_raw_buffer_load_b128(rws, hi + (k & 1) * 1024, (10 + 2 * t + (k >> 1)) * 2048, 16);
        SB();
      }
    }
#pragma unroll
    for (int k = 0; k < 8; ++k) {
      u32x4 v;
#pragma unroll
      for (int e = 0; e < 4; ++e) v[e] = pk_bf2(pr(acc[k >> 2][(k >> 1) & 1], 8 * (k & 1) + 2 * e));
      lds128w(LR + (unsigned)(k * 1024 + lane * 16), v);
    }
    SB();
    STAMP(19);
    const int rho = rgw & 1, kap = rgw >> 1;
    u32x4 I[15][2];
#pragma unroll
    for (int x = 0; x < 2; ++x) {
      const float d = (float)(64 * cg + 32 * x + l31), dl = d * (1.0f / 448.0f);
      const float tw = 2.0f * cosr(rev448(8.0f * d));
      const float k0 = (float)(112 * kap + 4 * h);
      const float a0 = rev448(k0 * d), a1 = rev448((k0 + 8.0f) * d);
      f32x2 f0[4], f1[4];
#pragma unroll
      for (int e = 0; e < 4; ++e) {
        const float b0 = __builtin_fmaf((float)e, dl, a0), b1 = __builtin_fmaf((float)e, dl, a1);
        f0[e >> 1][e & 1] = cosr(b0);
        f0[2 + (e >> 1)][e & 1] = sinr(-b0);
        f1[e >> 1][e & 1] = cosr(b1);
        f1[2 + (e >> 1)][e & 1] = sinr(-b1);
      }
#pragma unroll
      for (int i = 0; i < 15; ++i) {
#pragma unroll
        for (int e = 0; e < 4; ++e) {
          I[i][x][e] = pk_bf2(f0[e]);
          const f32x2 n = fma2(bc(tw), f1[e], -f0[e]);
          f0[e] = f1[e];
          f1[e] = n;
        }
        PIN(I[i][x]);
      }
    }
    u32x4 GC[2][2];
#pragma unroll
    for (int sh = 0; sh < 2; ++sh) {
      const float sf = (float)(32 * (32 * sh + l31) + s2) * (1.0f / 2048.0f);
#pragma unroll
      for (int t = 0; t < 2; ++t)
#pragma unroll
        for (int e = 0; e < 4; ++e) {
          const int p = 16 * rho + 8 * t + 4 * (e >> 1) + 2 * h + (e & 1);
          const float a = (float)p * sf;
          float sv = sinr(a);
          if (t == 0 && e == 0) sv = (p == 0) ? ((l31 & 1) ? -1.0f : 1.0f) : sv;
          GC[sh][t][e] = pk_bf(cosr(a), sv);
        }
      PIN(GC[sh][0]); PIN(GC[sh][1]);
    }
    unsigned ai = WS_A + (unsigned)(((s2 * 2 + rho) * 29 + 14 * kap) * 1024 + (l31 >> 3) * 256 + h * 128 + (l31 & 7) * 16);
    PIN(ai);
    STAMP(20);
    {
      const int gi = lane >> 2, wv = (gi & 7) == 7 ? ((kap == 1 && gi == 15) ? 7 : 6) : (gi & 7);
      poll(sws, WS_AF + (unsigned)(((4 * rho + (lane & 3)) * 4 + 2 * kap + (gi >> 3)) * 128 + wv * 8), tag);
    }
    STAMP(21);
    u32x4 A[15];
#pragma unroll
    for (int i = 0; i < 8; ++i) A[i] = __builtin_amdgcn_raw_buffer_load_b128(rws, ai, i * 1024, 16);
    SB();
    f32x16 T[2] = {};
#pragma unroll
    for (int i = 0; i < 15; ++i) {
      T[0] = mfma_bf(A[i], I[i][0], T[0]);
      T[1] = mfma_bf(A[i], I[i][1], T[1]);
      if (i < 3) {
#pragma unroll
        for (int k = 8 + 3 * i; k < 11 + 3 * i && k < 15; ++k)
          A[k] = __builtin_amdgcn_raw_buffer_load_b128(rws, (k == 14 && kap == 0) ? OOB : ai, k * 1024, 16);
        if (i == 2) {
          xres = __builtin_amdgcn_raw_buffer_load_b128(rx, oo, 0, 0);
          b2v = __builtin_amdgcn_raw_buffer_load_b128(rb2, (unsigned)(128 * cg + (tid & 7) * 16), 0, 0);
        }
        SB();
      }
      if (i >= 7) {
        const int k = i - 7;
        const u32x4 v = lds128(LR + (unsigned)(k * 1024 + lane * 16));
#pragma unroll
        for (int e = 0; e < 4; ++e) {
          acc[k >> 2][(k >> 1) & 1][8 * (k & 1) + 2 * e] = bf_lo(v[e]);
          acc[k >> 2][(k >> 1) & 1][8 * (k & 1) + 2 * e + 1] = bf_hi(v[e]);
        }
        SB();
      }
    }
#pragma unroll
    for (int x = 0; x < 2; ++x)
#pragma unroll
      for (int t = 0; t < 2; ++t) {
        u32x4 tf;
#pragma unroll
        for (int e = 0; e < 4; ++e) tf[e] = pk_bf2(pr(T[x], 8 * t + 2 * e));
        acc[x][0] = mfma_bf(tf, GC[0][t], acc[x][0]);
        acc[x][1] = mfma_bf(tf, GC[1][t], acc[x][1]);
      }
  } else {
    WAITVM(0);
    if (lane == 0) __builtin_amdgcn_raw_buffer_store_b64(tag, rws, hfo, 0, 16);
    STAMP(16);
#pragma unroll 1
    for (int n = 0; n < SPIN_CAP; ++n) {
      const unsigned dn = *(volatile LAS unsigned*)(LB + L_DONE + (lane & 7) * 4);
      if (__builtin_amdgcn_ballot_w64(dn == 0u) == 0) break;
    }
    asm volatile("" ::: "memory");
#pragma unroll
    for (int pc = 0; pc < 14; ++pc) w2dma(LQ + pc * 1024, pc);
    STAMP(17);
    poll(sws, hpo, tag);
    STAMP(18);
#pragma unroll
    for (int pc = 14; pc < 28; ++pc) w2dma(LR + (pc - 6) * 1024, pc);
    u32x4 H[14][2];
#pragma unroll
    for (int t = 0; t < 14; ++t)
#pragma unroll
      for (int sh = 0; sh < 2; ++sh) H[t][sh] = __builtin_amdgcn_raw_buffer_load_b128(rws, hi + sh * 1024, t * 2048, 16);
    xres = __builtin_amdgcn_raw_buffer_load_b128(rx, oo, 0, 0);
    b2v = __builtin_amdgcn_raw_buffer_load_b128(rb2, (unsigned)(128 * cg + (tid & 7) * 16), 0, 0);
    WAITVM(30);
    STAMP(19);
#pragma unroll
    for (int t = 0; t < 14; ++t)
#pragma unroll
      for (int x = 0; x < 2; ++x) {
        const unsigned a = (t < 7 ? LQ + t * 2048 : LR + (t - 3) * 2048) + (w2l ^ (64u * x));
        const u32x4 wf = trfrag(a, a + 1024);
        acc[x][0] = mfma_bf(wf, H[t][0], acc[x][0]);
        acc[x][1] = mfma_bf(wf, H[t][1], acc[x][1]);
      }
  }
  {
    const unsigned pb = (grp ? LQ : LR) + (unsigned)(l31 * 128 + h * 8);
#pragma unroll
    for (int x = 0; x < 2; ++x)
#pragma unroll
      for (int sh = 0; sh < 2; ++sh)
#pragma unroll
        for (int q = 0; q < 4; ++q) {
          const u32x2 v = {pk_bf2(pr(acc[x][sh], 4 * q)), pk_bf2(pr(acc[x][sh], 4 * q + 2))};
          *(LAS u32x2*)(pb + (unsigned)(sh * 4096 + (((4 * x + q) ^ (l31 & 7)) * 16))) = v;
        }
  }
  STAMP(22);
  LBAR();
  {
    const unsigned ra = LB + (unsigned)((tid >> 3) * 128 + (((tid & 7) ^ ((tid >> 3) & 7)) * 16));
    f32x2 r[4];
#pragma unroll
    for (int e = 0; e < 4; ++e) r[e] = f32x2{bf_lo(xres[e]), bf_hi(xres[e])} + f32x2{bf_lo(b2v[e]), bf_hi(b2v[e])};
#pragma unroll
    for (int k = 0; k < 8; ++k) {
      const u32x4 v = lds128(ra + (k < 4 ? L_C0 + k * 22528 : (k - 4) * 14336));
#pragma unroll
      for (int e = 0; e < 4; ++e) r[e] += f32x2{bf_lo(v[e]), bf_hi(v[e])};
    }
    const u32x4 o = {pk_bf2(r[0]), pk_bf2(r[1]), pk_bf2(r[2]), pk_bf2(r[3])};
    __builtin_amdgcn_raw_buffer_store_b128(o, rout, oo, 0, 16);
  }
  if (blockIdx.x == 0 && tid == 0) __builtin_amdgcn_raw_buffer_store_b64(tag, rws, WS_E, 0, 16);
  STAMP(23);
  STAMP_OUT();
}

extern "C" void kernel_launch(void* const* d_in, const int* in_sizes, int n_in, void* d_out, int out_size,
                              void* d_ws, size_t ws_size, hipStream_t stream) {
  fused_kernel<<<256, 512, 0, stream>>>(d_in[0], d_in[1], d_in[2], d_in[4], d_in[5], d_in[6], d_in[7], d_out, d_ws);
}
```

```cpp
#include <hip/hip_runtime.h>
#include <stdint.h>
#include <type_traits>
#pragma clang diagnostic ignored "-Wint-to-pointer-cast"

#define DEV __device__ __forceinline__
#define INL __attribute__((always_inline))
#if defined(__HIP_DEVICE_COMPILE__)
#define NOPK __attribute__((target("no-packed-fp32-ops")))
#else
#define NOPK
#endif
#define LAS __attribute__((address_space(3)))
typedef __attribute__((ext_vector_type(4))) short s16x4;
typedef __attribute__((ext_vector_type(8))) short s16x8;
typedef __attribute__((ext_vector_type(8))) __bf16 bf16x8;
typedef __attribute__((ext_vector_type(2))) __bf16 bf16x2;
typedef __attribute__((ext_vector_type(8))) _Float16 f16x8;
typedef __attribute__((ext_vector_type(2))) _Float16 f16x2;
typedef __attribute__((ext_vector_type(2))) float f32x2;
typedef __attribute__((ext_vector_type(4))) float f32x4;
typedef __attribute__((ext_vector_type(16))) float f32x16;
typedef __attribute__((ext_vector_type(2))) unsigned u32x2;
typedef __attribute__((ext_vector_type(4))) unsigned u32x4;
typedef __amdgpu_buffer_rsrc_t rsrc_t;

constexpr unsigned WS_E = 0;
constexpr unsigned WS_ZF = 4096;
constexpr unsigned WS_HF = WS_ZF + 224 * 128;
constexpr unsigned WS_AF = WS_HF + 224 * 128;
constexpr unsigned WS_CS = 65536;
constexpr unsigned WS_Z = 131072;
constexpr unsigned WS_A = WS_Z + 29 * 16 * 4096;
constexpr unsigned WS_H = WS_A + 32 * 2 * 29 * 1024;
constexpr unsigned WS_END = WS_H + 32 * 112 * 2048;
constexpr unsigned OOB = 0x80000000u;
constexpr int SPIN_CAP = 1 << 16;
#define CODE_BYTES 32768

constexpr unsigned L_C0 = 57344;
constexpr unsigned L_UH = 7 * 16384;
constexpr unsigned L_GATH = 8 * 16384;
constexpr unsigned L_STAT = 147456;
constexpr unsigned L_RROW = L_STAT + 4096;
constexpr unsigned L_DONE = L_RROW + 512;
DEV constexpr unsigned xblk(int kc) { return (unsigned)((0x8217034u >> (4 * kc)) & 15u) * 16384u; }

#define WAITBAR(n) asm volatile("s_waitcnt vmcnt(" #n ") lgkmcnt(0)\n\ts_barrier" ::: "memory")
#define LBAR() asm volatile("s_waitcnt lgkmcnt(0)\n\ts_barrier" ::: "memory")
#define PIN(x) asm volatile("" : "+v"(x))
#define SB() __builtin_amdgcn_sched_barrier(0)
#define STAMP(i)
#define STAMP_OUT()
#define WAITVM(n) asm volatile("s_waitcnt vmcnt(" #n ")" ::: "memory")

DEV u32x4 mk_srd(const void* p, unsigned bytes) {
  uint64_t a = (uint64_t)p;
  u32x4 r = {(unsigned)a, (unsigned)(a >> 32) & 0xffffu, bytes, 0x00020000u};
  return r;
}
DEV void dma16(unsigned lds, unsigned voff, u32x4 srd, unsigned soff) {
  asm volatile("s_mov_b32 m0, %0\n\ts_nop 0\n\tbuffer_load_dwordx4 %1, %2, %3 offen lds"
               :: "s"(lds), "v"(voff), "s"(srd), "s"(soff) : "memory");
}
DEV u32x4 lds128(unsigned a) { return *(LAS u32x4*)a; }
DEV void lds128w(unsigned a, u32x4 v) { *(LAS u32x4*)a = v; }
DEV u32x4 trfrag(unsigned a0, unsigned a1) {
  s16x4 lo = __builtin_amdgcn_ds_read_tr16_b64_v4i16((LAS s16x4*)a0);
  s16x4 hi = __builtin_amdgcn_ds_read_tr16_b64_v4i16((LAS s16x4*)a1);
  return __builtin_bit_cast(u32x4, __builtin_shufflevector(lo, hi, 0, 1, 2, 3, 4, 5, 6, 7));
}
DEV f32x16 mfma_bf(u32x4 a, u32x4 b, f32x16 c) {
  return __builtin_amdgcn_mfma_f32_32x32x16_bf16(__builtin_bit_cast(bf16x8, a), __builtin_bit_cast(bf16x8, b), c, 0, 0, 0);
}
DEV f32x16 mfma_h(u32x4 a, u32x4 b, f32x16 c) {
  return __builtin_amdgcn_mfma_f32_32x32x16_f16(__builtin_bit_cast(f16x8, a), __builtin_bit_cast(f16x8, b), c, 0, 0, 0);
}
DEV unsigned pk_bf(float lo, float hi) {
  f32x2 p = {lo, hi};
  return __builtin_bit_cast(unsigned, __builtin_convertvector(p, bf16x2));
}
DEV unsigned pk_h(float lo, float hi) {
  f32x2 p = {lo, hi};
  return __builtin_bit_cast(unsigned, __builtin_convertvector(p, f16x2));
}
DEV unsigned pk_bf2(f32x2 p) { return __builtin_bit_cast(unsigned, __builtin_convertvector(p, bf16x2)); }
DEV unsigned pk_h2(f32x2 p) { return __builtin_bit_cast(unsigned, __builtin_convertvector(p, f16x2)); }
DEV f32x2 pr(const f32x16& v, int r) { return f32x2{v[r], v[r + 1]}; }
DEV f32x2 fma2(f32x2 a, f32x2 b, f32x2 c) { return __builtin_elementwise_fma(a, b, c); }
DEV f32x2 bc(float a) { return f32x2{a, a}; }
DEV float bf_lo(unsigned v) { return __builtin_bit_cast(float, v << 16); }
DEV float bf_hi(unsigned v) { return __builtin_bit_cast(float, v & 0xffff0000u); }
DEV float bf16r(float x) { return bf_lo(pk_bf(x, 0.f)); }
DEV float dot2(unsigned a, unsigned b, float c) {
  return __builtin_amdgcn_fdot2_f32_bf16(__builtin_bit_cast(bf16x2, a), __builtin_bit_cast(bf16x2, b), c, false);
}
DEV float cosr(float rev) { return __builtin_amdgcn_cosf(rev); }
DEV float sinr(float rev) { return __builtin_amdgcn_sinf(rev); }
DEV float rev448(float n) {
  float q = __builtin_floorf(n * (1.0f / 448.0f));
  return __builtin_fmaf(q, -448.0f, n) * (1.0f / 448.0f);
}
DEV void poll(u32x4 srd, unsigned voff, u32x2 tag) {
#pragma unroll 1
  for (int n = 0; n < SPIN_CAP; ++n) {
    u32x2 v;
    asm volatile("buffer_load_dwordx2 %0, %1, %2, 0 offen sc1\n\ts_waitcnt vmcnt(0)"
                 : "=&v"(v) : "v"(voff), "s"(srd) : "memory");
    if (__builtin_amdgcn_ballot_w64(v[0] != tag[0] || v[1] != tag[1]) == 0) break;
    __builtin_amdgcn_s_sleep(1);
  }
}
DEV u32x2 mk_tag(u32x2 ev) {
  uint64_t e = ((uint64_t)__builtin_amdgcn_readfirstlane(ev[1]) << 32) | (unsigned)__builtin_amdgcn_readfirstlane(ev[0]);
  e = e * 6364136223846793005ull + 1442695040888963407ull;
  return u32x2{(unsigned)e, (unsigned)(e >> 32)};
}
template <int A, int B, class Fn> DEV void sfor(Fn&& f) {
  if constexpr (A < B) { f(std::integral_constant<int, A>{}); sfor<A + 1, B>(f); }
}
DEV int shx(int v, int m) { return __builtin_amdgcn_ds_bpermute((int)((__builtin_amdgcn_mbcnt_hi(~0u, __builtin_amdgcn_mbcnt_lo(~0u, 0u)) ^ m) << 2), v); }
DEV float xor_add(float v, int m) { return v + __builtin_bit_cast(float, shx(__builtin_bit_cast(int, v), m)); }
DEV int imin(int a, int b) { return a < b ? a : b; }

__global__ void NOPK __launch_bounds__(512, 2)
fused_kernel(const void* __restrict__ px, const void* __restrict__ pwr, const void* __restrict__ pwi,
             const void* __restrict__ pw1, const void* __restrict__ pb1, const void* __restrict__ pw2,
             const void* __restrict__ pb2, void* __restrict__ pout, void* __restrict__ pws) {
  __shared__ __attribute__((aligned(1024))) char smem[163840];
  const unsigned LB = (unsigned)(uintptr_t)(LAS char*)smem;
  const int tid = threadIdx.x;
  const int lane = tid & 63;
  const int w = __builtin_amdgcn_readfirstlane(tid >> 6);
  const int l31 = lane & 31, h = lane >> 5;
  const int xcd = blockIdx.x & 7, loc = blockIdx.x >> 3;

  const u32x4 sws = mk_srd(pws, WS_END);
  const rsrc_t rws = __builtin_amdgcn_make_buffer_rsrc(pws, 0, WS_END, 0x00020000);
  STAMP(0);
  unsigned pfv = 0;
  if (w >= 4) {
    const u32x4 sc = mk_srd((const void*)__builtin_amdgcn_s_getpc(), CODE_BYTES);
    asm volatile("buffer_load_dword %0, %1, %2, 0 offen" : "=v"(pfv) : "v"((tid - 256) * 128), "s"(sc) : "memory");
  }
  u32x2 ev;
  asm volatile("s_nop 4\n\tbuffer_load_dwordx2 %0, off, %1, 0 sc1" : "=v"(ev) : "s"(sws) : "memory");

  if (loc >= 28) {
    const int pq = xcd, qt = loc - 28;
    const int ch = tid & 7, rw = tid >> 3;
    const rsrc_t rw1 = __builtin_amdgcn_make_buffer_rsrc((void*)pw1, 0, 448 * 1792 * 2, 0x00020000);
    const rsrc_t rb1 = __builtin_amdgcn_make_buffer_rsrc((void*)pb1, 0, 1792 * 2, 0x00020000);
    const rsrc_t rwr = __builtin_amdgcn_make_buffer_rsrc((void*)pwr, 0, 2048 * 225 * 2, 0x00020000);
    const rsrc_t rwi = __builtin_amdgcn_make_buffer_rsrc((void*)pwi, 0, 2048 * 225 * 2, 0x00020000);
    const int col0 = 896 * (xcd & 1) + 224 * (xcd >> 1) + 56 * qt;
    u32x4 cw[7];
    {
      const unsigned o = (ch < 7) ? (unsigned)(rw * 3584 + col0 * 2 + ch * 16) : OOB;
#pragma unroll
      for (int i = 0; i < 7; ++i) cw[i] = __builtin_amdgcn_raw_buffer_load_b128(rw1, o, i * 64 * 3584, 0);
    }
    const unsigned b1v = __builtin_amdgcn_raw_buffer_load_b16(rb1, tid < 56 ? (unsigned)(col0 + tid) * 2u : OOB, 0, 0);
    u32x4 wv[8];
    unsigned wx[8];
#pragma unroll
    for (int i = 0; i < 4; ++i) {
      const int idx = rw + 64 * (i & 1), p = 4 * pq + (idx & 3), k2 = idx >> 2;
      const int k = (i >> 1) ? (p ? ((2048 - p + 64 * k2) & 2047) : (32 + 64 * k2)) : (p + 64 * k2);
      const unsigned o = (unsigned)(k * 225 + 56 * qt + 8 * ch) * 2u;
      const unsigned o4 = (ch < 7) ? o : OOB, o1 = (ch == 7 && qt == 3) ? o : OOB;
      wv[i] = __builtin_amdgcn_raw_buffer_load_b128(rwr, o4, 0, 0);
      wv[4 + i] = __builtin_amdgcn_raw_buffer_load_b128(rwi, o4, 0, 0);
      wx[i] = __builtin_amdgcn_raw_buffer_load_b16(rwr, o1, 0, 0);
      wx[4 + i] = __builtin_amdgcn_raw_buffer_load_b16(rwi, o1, 0, 0);
    }
    SB();
    {
      f32x2 cs[4] = {};
#pragma unroll
      for (int i = 0; i < 7; ++i)
#pragma unroll
        for (int e = 0; e < 4; ++e) cs[e] += f32x2{bf_lo(cw[i][e]), bf_hi(cw[i][e])};
      float c8[8];
#pragma unroll
      for (int e = 0; e < 8; ++e) c8[e] = xor_add(xor_add(xor_add(cs[e >> 1][e & 1], 8), 16), 32);
      if (lane < 8) {
        *(LAS f32x4*)(LB + 81920u + (unsigned)(w * 64 + lane * 8) * 4u) = f32x4{c8[0], c8[1], c8[2], c8[3]};
        *(LAS f32x4*)(LB + 81920u + (unsigned)(w * 64 + lane * 8) * 4u + 16u) = f32x4{c8[4], c8[5], c8[6], c8[7]};
      }
    }
    LBAR();
    asm volatile("" : "+v"(ev), "+v"(pfv));
    const u32x2 tag = mk_tag(ev);
    if (w == 0) {
      float cs = 0.f;
#pragma unroll
      for (int k = 0; k < 8; ++k) cs += *(LAS float*)(LB + 81920u + (unsigned)(k * 64 + lane) * 4u);
      const float hi = bf16r(cs);
      const u32x4 gr = {pk_bf(hi, cs - hi), tag[0], b1v, tag[1]};
      __builtin_amdgcn_raw_buffer_store_b128(gr, rws, lane < 56 ? WS_CS + (unsigned)(col0 + lane) * 16u : OOB, 0, 16);
    }
    STAMP(1);
#pragma unroll
    for (int i = 0; i < 8; ++i) {
      wv[i][0] |= wx[i];
      lds128w(LB + (unsigned)(((i >> 1) * 128 + rw + 64 * (i & 1)) * 144 + ch * 16), wv[i]);
    }
    const bool active = (w < 7) || (qt == 3);
    const int g = 7 * qt + w;
    const int kdl = lane & 7, pl = (lane >> 3) & 3;
    const int p = 4 * pq + pl, kd = 8 * g + kdl;
    u32x4 FR[4], FI[4], IR[4], II[4];
#pragma unroll
    for (int t = 0; t < 4; ++t) {
      float c[4], sn[4];
#pragma unroll
      for (int e = 0; e < 4; ++e) {
        const float a = (float)(l31 * (8 * t + 4 * h + e)) * (1.0f / 32.0f);
        c[e] = cosr(a);
        sn[e] = sinr(a);
        FR[t][e] = pk_h(c[e], sn[e]);
        FI[t][e] = pk_h(-sn[e], c[e]);
      }
      IR[t] = u32x4{pk_bf(c[0], c[1]), pk_bf(c[2], c[3]), pk_bf(-sn[0], -sn[1]), pk_bf(-sn[2], -sn[3])};
      II[t] = u32x4{pk_bf(sn[0], sn[1]), pk_bf(sn[2], sn[3]), pk_bf(c[0], c[1]), pk_bf(c[2], c[3])};
      PIN(FR[t]); PIN(FI[t]); PIN(IR[t]); PIN(II[t]);
    }
    LBAR();
    if (active) {
      f32x2 wpr[8], wpi[8], wmr[8], wmi[8];
      const float wsc = ((kd == 0 || kd == 224) ? 1.0f : 2.0f) / (2048.0f * 448.0f);
      const unsigned wa = LB + (unsigned)((16 * h + pl) * 144 + (8 * w + kdl) * 2);
#pragma unroll
      for (int r = 0; r < 16; ++r) {
        const unsigned o = (unsigned)(((r & 3) + 8 * (r >> 2)) * 4 * 144);
        wpr[r >> 1][r & 1] = bf_lo(*(LAS unsigned short*)(wa + o));
        wmr[r >> 1][r & 1] = bf_lo(*(LAS unsigned short*)(wa + o + 128 * 144));
        wpi[r >> 1][r & 1] = bf_lo(*(LAS unsigned short*)(wa + o + 256 * 144));
        wmi[r >> 1][r & 1] = bf_lo(*(LAS unsigned short*)(wa + o + 384 * 144));
      }
      float ff = pq ? 1.0f : 0.0f;
      float cph = pq ? 1.0f : cosr((float)l31 * (1.0f / 64.0f));
      float sph = pq ? 0.0f : sinr((float)l31 * (1.0f / 64.0f));
      PIN(ff); PIN(cph); PIN(sph);
#pragma unroll
      for (int r = 0; r < 8; ++r) {
        wpr[r] *= bc(wsc); wpi[r] *= bc(wsc); wmr[r] *= bc(wsc); wmi[r] *= bc(wsc);
        PIN(wpr[r]); PIN(wpi[r]); PIN(wmr[r]); PIN(wmi[r]);
      }
      unsigned zo = (kd > 224) ? OOB : WS_Z + (unsigned)((g * 16 + 2 * h) * 4096 + p * 128 + kdl * 16);
      unsigned ao = WS_A + (unsigned)((((lane >> 4) * 2 + (pq >> 2)) * 29 + g) * 1024 + (pq & 3) * 256 + (lane & 15) * 16);
      unsigned tw = LB + 90112u + (unsigned)(w * 8192 + l31 * 256);
      unsigned tr = LB + 90112u + (unsigned)(w * 8192 + (lane >> 4) * 256);
      PIN(zo); PIN(ao); PIN(tw); PIN(tr);
      STAMP(2);
      const int jz = (g == 28) ? 0 : (g >> 1);
      poll(sws, WS_ZF + (unsigned)(((lane & 15) * 14 + jz) * 128 + (pq >> 1) * 8), tag);
      STAMP(3);

      u32x4 z[8];
#pragma unroll
      for (int i = 0; i < 8; ++i)
        z[i] = __builtin_amdgcn_raw_buffer_load_b128(rws, zo, (4 * (i >> 1) + (i & 1)) * 4096, 16);
      SB();
      f32x16 xpr = {}, xpi = {}, xmr = {}, xmi = {};
#pragma unroll
      for (int t = 0; t < 4; ++t) {
        const u32x4 zp = {z[2 * t][0], z[2 * t][1], z[2 * t + 1][0], z[2 * t + 1][1]};
        xpr = mfma_h(FR[t], zp, xpr);
        xpi = mfma_h(FI[t], zp, xpi);
      }
#pragma unroll
      for (int t = 0; t < 4; ++t) {
        const u32x4 zm = {z[2 * t][2], z[2 * t][3], z[2 * t + 1][2], z[2 * t + 1][3]};
        xmr = mfma_h(FR[t], zm, xmr);
        xmi = mfma_h(FI[t], zm, xmi);
      }
      STAMP(4);
      f32x16 ypr = {}, ypi = {}, ymr = {}, ymi = {};
#pragma unroll
      for (int t = 0; t < 4; ++t) {
        u32x4 pp;
#pragma unroll
        for (int e = 0; e < 2; ++e) {
          const int r = 4 * t + 2 * e, q = 2 * t + e;
          pp[e] = pk_bf2(fma2(pr(xpr, r), wpr[q], -(pr(xpi, r) * wpi[q])));
          pp[2 + e] = pk_bf2(fma2(pr(xpr, r), wpi[q], pr(xpi, r) * wpr[q]));
        }
        ypr = mfma_bf(pp, IR[t], ypr);
        ypi = mfma_bf(pp, II[t], ypi);
      }
#pragma unroll
      for (int t = 0; t < 4; ++t) {
        u32x4 pm;
#pragma unroll
        for (int e = 0; e < 2; ++e) {
          const int r = 4 * t + 2 * e, q = 2 * t + e;
          pm[e] = pk_bf2(fma2(pr(xmr, r), wmr[q], -(pr(xmi, r) * wmi[q])));
          pm[2 + e] = pk_bf2(fma2(pr(xmr, r), wmi[q], pr(xmi, r) * wmr[q]));
        }
        ymr = mfma_bf(pm, IR[t], ymr);
        ymi = mfma_bf(pm, II[t], ymi);
      }
      STAMP(5);
#pragma unroll
      for (int q = 0; q < 4; ++q) {
        u32x4 uc, us;
#pragma unroll
        for (int e = 0; e < 2; ++e) {
          const int r = 4 * q + 2 * e;
          f32x2 acr, aci, asr, asi;
          if (q == 0) {
            acr = fma2(bc(ff), pr(ymr, r), pr(ypr, r));
            aci = fma2(bc(ff), pr(ymi, r), pr(ypi, r));
            asr = fma2(bc(cph), pr(ymi, r), fma2(bc(sph), pr(ymr, r), -(bc(ff) * pr(ypi, r))));
            asi = fma2(bc(sph), pr(ymi, r), fma2(bc(ff), pr(ypr, r), -(bc(cph) * pr(ymr, r))));
          } else {
            acr = pr(ypr, r) + pr(ymr, r);
            aci = pr(ypi, r) + pr(ymi, r);
            asr = pr(ymi, r) - pr(ypi, r);
            asi = pr(ypr, r) - pr(ymr, r);
          }
          uc[e] = pk_bf2(acr); uc[2 + e] = pk_bf2(aci);
          us[e] = pk_bf2(asr); us[2 + e] = pk_bf2(asi);
        }
        lds128w(tw + (unsigned)(((8 * h + 2 * q) ^ (l31 & 7)) * 16), uc);
        lds128w(tw + (unsigned)(((8 * h + 2 * q + 1) ^ (l31 & 7)) * 16), us);
      }
#pragma unroll
      for (int i = 0; i < 8; ++i) {
        const u32x4 v = lds128(tr + (unsigned)(i * 1024 + (((lane & 15) ^ ((4 * i + (lane >> 4)) & 7)) * 16)));
        __builtin_amdgcn_raw_buffer_store_b128(v, rws, ao + i * 8 * 29 * 1024, 0, 16);
      }
      STAMP(6);
      WAITVM(0);
      if (lane == 0) __builtin_amdgcn_raw_buffer_store_b64(tag, rws, WS_AF + (unsigned)((pq * 4 + qt) * 128 + w * 8), 0, 16);
      STAMP(7);
    }
    STAMP_OUT();
    return;
  }

  const int rg = 4 * (xcd >> 1) + loc / 7, j = 7 * (xcd & 1) + loc % 7;
  const int s2 = 4 * xcd + loc / 7, cg = loc % 7;
  const int rgw = w & 3, grp = w >> 2;
  const u32x4 sx = mk_srd(px, 2048 * 448 * 2), sw1 = mk_srd(pw1, 448 * 1792 * 2), sw2 = mk_srd(pw2, 448 * 1792 * 2);
  const unsigned vx = (unsigned)((32 * (8 * rgw + (lane >> 3)) + 2 * rg) * 896 + (((lane & 7) ^ ((4 * rgw + (lane >> 4)) & 7)) * 16));
  auto xdma = [&](int kc) INL {
#pragma unroll
    for (int q = 0; q < 4; ++q)
      dma16(LB + xblk(kc) + q * 4096 + rgw * 1024, vx, sx, kc * 128 + (q & 1) * 32 * 32 * 896 + (q >> 1) * 896);
  };
  const unsigned swz16 = (unsigned)(((lane & 7) ^ ((lane >> 2) & 4)) * 16);
  const unsigned vw1 = (unsigned)(lane >> 3) * 3584u + swz16;
  auto w1dma = [&](int c, int q) INL {
    dma16(LB + (c ? 0u : L_C0) + q * 1024, vw1, sw1, q * 8 * 3584 + 256 * j + 128 * c);
  };
  if (grp == 0) { xdma(0); xdma(1); xdma(2); xdma(3); }
  if (tid < 8) *(LAS unsigned*)(LB + L_DONE + tid * 4) = 0u;
  STAMP(1);

  u32x4 F[14];
  {
    const bool sp = (j == 0) && (l31 == 16);
    const float kdf = sp ? 224.0f : (float)(16 * j + (l31 & 15));
    const float ph = ((l31 >> 4) && !sp) ? 0.25f : 0.0f;
    const float tw = 2.0f * cosr(rev448(32.0f * kdf));
    const float d0 = (float)(16 * grp + 8 * h), dl = kdf * (1.0f / 448.0f);
    const float a0 = rev448(d0 * kdf) + ph, a1 = rev448((d0 + 32.0f) * kdf) + ph;
    f32x2 f0[4], f1[4];
#pragma unroll
    for (int e = 0; e < 8; ++e) {
      f0[e >> 1][e & 1] = cosr(__builtin_fmaf((float)e, dl, a0));
      f1[e >> 1][e & 1] = cosr(__builtin_fmaf((float)e, dl, a1));
    }
#pragma unroll
    for (int i = 0; i < 14; ++i) {
#pragma unroll
      for (int e = 0; e < 4; ++e) {
        F[i][e] = pk_bf2(f0[e]);
        const f32x2 n = fma2(bc(tw), f1[e], -f0[e]);
        f0[e] = f1[e];
        f1[e] = n;
      }
      PIN(F[i]);
    }
  }
  u32x4 GA[2][2] = {};
  float cph[2] = {}, sph[2] = {};
  if (grp == 1) {
    const int hh = (l31 >> 2) & 1, reg = (l31 & 3) + 4 * (l31 >> 3);
    const int s2a = 2 * rg + (rgw >> 1);
#pragma unroll
    for (int mt = 0; mt < 2; ++mt) {
      const int p = 16 * mt + 8 * hh + (reg >> 1), cs = reg & 1;
      const bool sp = (p == 0) && cs;
      const float ca = sp ? 0.5f : (float)p * (1.0f / 64.0f);
      const float cb = __builtin_fmaf(ca, (float)(32 * (rgw & 1) + 4 * h), sp ? 0.0f : (float)(p * s2a) * (1.0f / 2048.0f) - 0.25f * (float)cs);
      const float tw = 2.0f * cosr(8.0f * ca);
      f32x2 g0[2], g1[2];
#pragma unroll
      for (int e = 0; e < 4; ++e) {
        g0[e >> 1][e & 1] = cosr(__builtin_fmaf(ca, (float)e, cb));
        g1[e >> 1][e & 1] = cosr(__builtin_fmaf(ca, (float)(e + 8), cb));
      }
#pragma unroll
      for (int e = 0; e < 2; ++e) {
        const f32x2 g2 = fma2(bc(tw), g1[e], -g0[e]), g3 = fma2(bc(tw), g2, -g1[e]);
        GA[mt][0][e] = pk_h2(g0[e]); GA[mt][0][2 + e] = pk_h2(g1[e]);
        GA[mt][1][e] = pk_h2(g2); GA[mt][1][2 + e] = pk_h2(g3);
      }
      PIN(GA[mt][0]); PIN(GA[mt][1]);
    }
#pragma unroll
    for (int sg = 0; sg < 2; ++sg) {
      const float ang = ((tid & 255) >> 3) ? 0.0f : (float)(2 * rg + sg) * (1.0f / 64.0f);
      cph[sg] = cosr(ang); sph[sg] = sinr(ang);
      PIN(cph[sg]); PIN(sph[sg]);
    }
    xdma(4); xdma(5); xdma(6);
#pragma unroll
    for (int q = 0; q < 8; ++q) w1dma(0, 24 + 4 * q + rgw);
  }
  STAMP(2);

  u32x4 X[28];
  f32x16 U = {};
  float sum0 = 0.f, sum1 = 0.f, sq0 = 0.f, sq1 = 0.f;
  const unsigned xr = LB + (unsigned)(32 * rgw + l31) * 128u;
  const unsigned xs = (unsigned)((2 * grp + h) ^ ((l31 >> 1) & 7));
  auto xread = [&](int u) INL { X[u] = lds128(xr + ((xs ^ (2 * (u & 3))) * 16) + xblk(u >> 2)); };
  auto xstat = [&](int u) INL {
    sum0 = dot2(X[u][0], 0x3f803f80u, sum0); sq0 = dot2(X[u][0], X[u][0], sq0);
    sum1 = dot2(X[u][1], 0x3f803f80u, sum1); sq1 = dot2(X[u][1], X[u][1], sq1);
    sum0 = dot2(X[u][2], 0x3f803f80u, sum0); sq0 = dot2(X[u][2], X[u][2], sq0);
    sum1 = dot2(X[u][3], 0x3f803f80u, sum1); sq1 = dot2(X[u][3], X[u][3], sq1);
    PIN(sum0); PIN(sum1); PIN(sq0); PIN(sq1);
  };
  if (grp == 0) WAITVM(8);
  LBAR();
  STAMP(3);
#pragma unroll
  for (int u = 0; u < 8; u += 2) xread(u);
#pragma unroll
  for (int u = 1; u < 8; u += 2) xread(u);
  SB();
#pragma unroll
  for (int u = 0; u < 8; u += 2) { U = mfma_bf(X[u], F[u >> 1], U); xstat(u); SB(); }
  if (grp == 0) WAITVM(0);
  LBAR();
  STAMP(4);
#pragma unroll
  for (int u = 8; u < 16; u += 2) xread(u);
#pragma unroll
  for (int u = 9; u < 16; u += 2) xread(u);
  if (grp == 0) {
#pragma unroll
    for (int q = 0; q < 6; ++q) w1dma(0, 4 * q + rgw);
  } else {
    w1dma(1, 48 + rgw); w1dma(1, 52 + rgw);
  }
  SB();
#pragma unroll
  for (int u = 8; u < 16; u += 2) { U = mfma_bf(X[u], F[u >> 1], U); xstat(u); SB(); }
  if (grp == 1) WAITVM(10);
  LBAR();
  STAMP(5);
#pragma unroll
  for (int u = 16; u < 28; u += 2) xread(u);
#pragma unroll
  for (int u = 17; u < 28; u += 2) xread(u);
  w1dma(1, w); w1dma(1, 8 + w);
  SB();
#pragma unroll
  for (int u = 16; u < 28; u += 2) { U = mfma_bf(X[u], F[u >> 1], U); xstat(u); SB(); }
  *(LAS f32x2*)(LB + L_STAT + (unsigned)((32 * rgw + l31) * 4 + 2 * grp + h) * 8u) = f32x2{sum0 + sum1, sq0 + sq1};
  if (grp == 0) {
#pragma unroll
    for (int q = 0; q < 4; ++q)
      *(LAS f32x4*)(LB + L_UH + (unsigned)(rgw * 4096 + q * 1024 + lane * 16)) = f32x4{U[4 * q], U[4 * q + 1], U[4 * q + 2], U[4 * q + 3]};
  } else {
    WAITVM(4);
  }
  STAMP(6);
  LBAR();
  STAMP(7);
  u32x4 cg0, cg1;
  const unsigned cso = WS_CS + (unsigned)(128 * j + 64 * grp + l31) * 16u;
  asm volatile("buffer_load_dwordx4 %0, %2, %3, 0 offen sc1\n\tbuffer_load_dwordx4 %1, %2, %3, 0 offen offset:512 sc1"
               : "=&v"(cg0), "=&v"(cg1) : "v"(cso), "s"(sws) : "memory");
  asm volatile("" : "+v"(ev), "+v"(pfv));
  const u32x2 tag = mk_tag(ev);
  float sig, rstd;
  u32x4 xaug;
  {
    const f32x4 a = *(LAS f32x4*)(LB + L_STAT + (unsigned)(32 * rgw + l31) * 32u);
    const f32x4 b = *(LAS f32x4*)(LB + L_STAT + (unsigned)(32 * rgw + l31) * 32u + 16u);
    const f32x2 t = (f32x2{a[0], a[1]} + f32x2{a[2], a[3]}) + (f32x2{b[0], b[1]} + f32x2{b[2], b[3]});
    const float mean = t[0] * (1.0f / 448.0f);
    const float var = __builtin_fmaxf(__builtin_fmaf(-mean, mean, t[1] * (1.0f / 448.0f)), 0.0f);
    sig = bf16r(__builtin_amdgcn_sqrtf(bf16r(bf16r(var) + 1e-5f)));
    rstd = __builtin_amdgcn_rcpf(sig);
    xaug = u32x4{pk_bf(-mean, -mean), pk_bf(sig, 0.f), 0u, 0u};
  }
  if (grp == 1) {
    __builtin_amdgcn_s_setprio(2);
    *(LAS float*)(LB + L_RROW + (unsigned)(rgw * 32 + l31) * 4u) = rstd;
    const u32x4 faug = {(j == 0 && lane == 0) ? 0x43e0u : 0u, 0u, 0u, 0u};
    U = mfma_bf(xaug, faug, U);
    u32x4 ub[2];
#pragma unroll
    for (int q = 0; q < 4; ++q) {
      const f32x4 uo = *(LAS f32x4*)(LB + L_UH + (unsigned)(rgw * 4096 + q * 1024 + lane * 16));
      const f32x4 rr = *(LAS f32x4*)(LB + L_RROW + (unsigned)(rgw * 32 + 8 * q + 4 * h) * 4u);
      ub[q >> 1][2 * (q & 1)] = pk_h2((pr(U, 4 * q) + f32x2{uo[0], uo[1]}) * f32x2{rr[0], rr[1]});
      ub[q >> 1][2 * (q & 1) + 1] = pk_h2((pr(U, 4 * q + 2) + f32x2{uo[2], uo[3]}) * f32x2{rr[2], rr[3]});
    }
#pragma unroll
    for (int mt = 0; mt < 2; ++mt) {
      f32x16 za = {};
      za = mfma_h(GA[mt][0], ub[0], za);
      za = mfma_h(GA[mt][1], ub[1], za);
#pragma unroll
      for (int q = 0; q < 2; ++q) {
        u32x4 v;
#pragma unroll
        for (int e = 0; e < 4; ++e) v[e] = pk_h2(pr(za, 8 * q + 2 * e));
        lds128w(LB + L_GATH + (unsigned)(rgw * 4096 + l31 * 128 + (((4 * mt + 2 * h + q) ^ (l31 & 7)) * 16)), v);
      }
    }
    STAMP(8);
    LBAR();
    STAMP(9);
    const int p = (tid & 255) >> 3, kdl = tid & 7;
    const unsigned ga = LB + L_GATH + (unsigned)(kdl * 128 + (((p >> 2) ^ kdl) * 16) + (p & 3) * 4);
    const float ff = p ? 1.0f : 0.0f;
    u32x4 zx;
#pragma unroll
    for (int gl = 0; gl < 2; ++gl) {
      const bool sp = (gl == 0) && (j == 0) && (kdl == 0);
      u32x4 z;
#pragma unroll
      for (int sg = 0; sg < 2; ++sg) {
        float v[4];
#pragma unroll
        for (int uv = 0; uv < 2; ++uv) {
          const f16x2 ts = *(LAS f16x2*)(ga + (unsigned)(2 * sg * 4096 + uv * 2048 + gl * 1024)) +
                           *(LAS f16x2*)(ga + (unsigned)((2 * sg + 1) * 4096 + uv * 2048 + gl * 1024));
          v[2 * uv] = (float)ts[0];
          v[2 * uv + 1] = (float)ts[1];
        }
        const float a = v[0], e = v[1], c = sp ? 0.f : v[2], b = sp ? 0.f : v[3];
        float o[4] = {a + ff * b, c - ff * e, ff * a - (cph[sg] * b - sph[sg] * e), ff * c + (cph[sg] * e + sph[sg] * b)};
#pragma unroll
        for (int k = 0; k < 4; ++k) asm("" : "+v"(o[k]));
        z[sg] = pk_h(o[0], o[1]);
        z[2 + sg] = pk_h(o[2], o[3]);
        if (gl == 0) {
          float q[3] = {-ff * v[3], ff * v[2] + sph[sg] * v[3], cph[sg] * v[3]};
#pragma unroll
          for (int k = 0; k < 3; ++k) asm("" : "+v"(q[k]));
          zx[sg] = pk_h(v[2], q[0]);
          zx[2 + sg] = pk_h(q[1], q[2]);
        }
      }
      __builtin_amdgcn_raw_buffer_store_b128(z, rws, WS_Z + (unsigned)(((2 * j + gl) * 16 + rg) * 4096 + (tid & 255) * 16), 0, 16);
    }
    __builtin_amdgcn_raw_buffer_store_b128(zx, rws, (j == 0 && kdl == 0) ? WS_Z + (unsigned)((28 * 16 + rg) * 4096 + p * 128) : OOB, 0, 16);
    __builtin_amdgcn_s_setprio(0);
    STAMP(10);
    WAITBAR(3);
  }
  STAMP(11);

  const int tq = (lane >> 2) & 3, tp = lane & 3, tg = (lane >> 4) & 1;
  const unsigned w1b = LB + (grp ? 0u : L_C0) + (unsigned)((8 * h + tq) * 128 + 64 * (tq >> 1) + 32 * tg + 8 * tp);
  const unsigned w1e = w1b + 2048u * grp, w1o = w1b + 2048u * (1 - grp);
  auto w1frag = [&](int c, int u) INL {
    const unsigned a = (((u & 1) ? w1o : w1e) ^ (64u * c)) + 2048u * (u & ~1);
    return trfrag(a, a + 512);
  };
  const float gc1 = -2.302208198f * rstd, gc2 = gc1 * 0.044715f * rstd * rstd;
  const unsigned ho = WS_H + (unsigned)((((2 * rg + (rgw >> 1)) * 112 + 56 * grp + 4 * j) * 2 + (rgw & 1)) * 1024 + lane * 16);
  auto gelu2 = [&](f32x2 u) INL {
    const f32x2 ar = fma2(u * u, bc(gc2), bc(gc1)) * u;
    const f32x2 ex = {__builtin_amdgcn_exp2f(ar[0]), __builtin_amdgcn_exp2f(ar[1])};
    const f32x2 dn = fma2(ex, bc(sig), bc(sig));
    return pk_bf2(u * f32x2{__builtin_amdgcn_rcpf(dn[0]), __builtin_amdgcn_rcpf(dn[1])});
  };
  f32x16 h0 = {}, h1 = {};
#define SEQ_C(i) ((i) < 16 ? 0 : (i) < 24 ? 1 : (i) < 36 ? 0 : 1)
#define SEQ_U(i) ((i) < 16 ? 12 + (i) : (i) < 24 ? (i) - 4 : (i) < 36 ? (i) - 24 : (i) < 44 ? (i) - 16 : (i) - 44)
  {
    u32x4 wf[3];
    wf[0] = w1frag(SEQ_C(0), SEQ_U(0));
    wf[1] = w1frag(SEQ_C(1), SEQ_U(1));
    u32x4 ho0[2];
sfor<0, 56>([&](auto ic) INL {
      constexpr int i = ic;
      if (i == 22 && grp == 0) { STAMP(8); WAITBAR(12); STAMP(9); }
      if (i == 8 && grp == 1) {
        WAITVM(0);
        if (lane == 0) __builtin_amdgcn_raw_buffer_store_b64(tag, rws, WS_ZF + (unsigned)((rg * 14 + j) * 128 + rgw * 8), 0, 16);
        STAMP(12);
      }
      if constexpr (i + 2 < 56) wf[(i + 2) % 3] = w1frag(SEQ_C(i + 2), SEQ_U(i + 2));
      if (SEQ_C(i) == 0) h0 = mfma_bf(wf[i % 3], X[SEQ_U(i)], h0);
      else h1 = mfma_bf(wf[i % 3], X[SEQ_U(i)], h1);
      if (i < 16 && (i & 3) == 1 && grp == 0) { w1dma(1, 16 + 2 * (i & 12) + rgw); w1dma(1, 20 + 2 * (i & 12) + rgw); }
      if (i == 34) {
        WAITVM(8);
        asm volatile("" : "+v"(cg0), "+v"(cg1));
#pragma unroll 1
        for (int n = 0; n < SPIN_CAP; ++n) {
          if (__builtin_amdgcn_ballot_w64(cg0[1] != tag[0] || cg0[3] != tag[1] || cg1[1] != tag[0] || cg1[3] != tag[1]) == 0) break;
          asm volatile("buffer_load_dwordx4 %0, %2, %3, 0 offen sc1\n\tbuffer_load_dwordx4 %1, %2, %3, 0 offen offset:512 sc1\n\ts_waitcnt vmcnt(0)"
                       : "=&v"(cg0), "=&v"(cg1) : "v"(cso), "s"(sws) : "memory");
        }
        cg0 = u32x4{h ? 0u : cg0[0], h ? 0u : cg0[2], 0u, 0u};
        cg1 = u32x4{h ? 0u : cg1[0], h ? 0u : cg1[2], 0u, 0u};
      }
      if (i == 35) h0 = mfma_bf(cg0, xaug, h0);
      if (i == 36) h1 = mfma_bf(cg1, xaug, h1);
      if constexpr (i >= 38 && i < 54 && !(i & 1)) {
        constexpr int e = (i - 38) >> 1;
        ho0[e >> 2][e & 3] = gelu2(pr(h0, 2 * e));
        if ((e & 3) == 3) __builtin_amdgcn_raw_buffer_store_b128(ho0[e >> 2], rws, ho + (e >> 2) * 2048, 0, 16);
      }
      if (i < 38 || (i & 1)) SB();
    });
  }
  STAMP(13);
  const unsigned vw2 = (unsigned)(lane >> 3) * 896u + swz16;
  auto w2dma = [&](unsigned lds, int pc) INL {
    const int ks = 14 * rgw + (pc >> 1);
    dma16(lds, vw2, sw2, (128 * (ks >> 2) + 64 * grp + 16 * (ks & 3) + 8 * (pc & 1)) * 896 + 128 * cg);
  };
  const unsigned LR = LB + L_C0 + rgw * 22528, LQ = LB + rgw * 14336;
  if (grp == 0) {
    WAITBAR(2);
#pragma unroll
    for (int pc = 0; pc < 8; ++pc) w2dma(LR + pc * 1024, pc);
  } else {
    asm volatile("s_waitcnt lgkmcnt(0)" ::: "memory");
    if (lane == 0) *(LAS unsigned*)(LB + L_DONE + w * 4) = 1u;
  }
  SB();
  STAMP(14);
#pragma unroll
  for (int t = 0; t < 2; ++t) {
    u32x4 o;
#pragma unroll
    for (int e = 0; e < 4; ++e) o[e] = gelu2(pr(h1, 8 * t + 2 * e));
    __builtin_amdgcn_raw_buffer_store_b128(o, rws, ho + 4096 + t * 2048, 0, 16);
  }
  SB();
  STAMP(15);

  const rsrc_t rx = __builtin_amdgcn_make_buffer_rsrc((void*)px, 0, 2048 * 448 * 2, 0x00020000);
  const rsrc_t rb2 = __builtin_amdgcn_make_buffer_rsrc((void*)pb2, 0, 448 * 2, 0x00020000);
  const rsrc_t rout = __builtin_amdgcn_make_buffer_rsrc(pout, 0, 2048 * 448 * 2, 0x00020000);
  const unsigned hfo = WS_HF + (unsigned)((rg * 14 + j) * 128 + w * 8);
  const unsigned hpo = WS_HF + (unsigned)(((s2 >> 1) * 14 + imin((14 * rgw + 4 * ((lane >> 1) & 3)) >> 2, (14 * rgw + 13) >> 2)) * 128 +
                                          (4 * grp + 2 * (s2 & 1) + (lane & 1)) * 8);
  const unsigned hi = WS_H + (unsigned)((s2 * 112 + 56 * grp + 14 * rgw) * 2048 + lane * 16);
  const unsigned w2l = (unsigned)((4 * h + tq) * 128 + 64 * (tq >> 1) + 32 * tg + 8 * tp);
  const unsigned oo = (unsigned)((32 * (tid >> 3) + s2) * 896 + 128 * cg + (tid & 7) * 16);
  u32x4 xres, b2v;
  f32x16 acc[2][2] = {};
  if (grp == 0) {
#pragma unroll
    for (int pc = 8; pc < 22; ++pc) w2dma(LR + pc * 1024, pc);
    WAITVM(14);
    if (lane == 0) __builtin_amdgcn_raw_buffer_store_b64(tag, rws, hfo, 0, 16);
    STAMP(16);
    WAITVM(1);
    u32x4 W2R[11][2];
#pragma unroll
    for (int t = 0; t < 11; ++t)
#pragma unroll
      for (int x = 0; x < 2; ++x) {
        const unsigned a = LR + (w2l ^ (64u * x)) + t * 2048;
        W2R[t][x] = trfrag(a, a + 1024);
      }
    asm volatile("s_waitcnt lgkmcnt(0)" ::: "memory");
    if (lane == 0) *(LAS unsigned*)(LB + L_DONE + w * 4) = 1u;
#pragma unroll
    for (int pc = 22; pc < 28; ++pc) w2dma(LR + (pc - 22) * 1024, pc);
    STAMP(17);
    poll(sws, hpo, tag);
    STAMP(18);
    u32x4 H[14][2];
#pragma unroll
    for (int t = 0; t < 10; ++t)
#pragma unroll
      for (int sh = 0; sh < 2; ++sh) H[t][sh] = __builtin_amdgcn_raw_buffer_load_b128(rws, hi + sh * 1024, t * 2048, 16);
    SB();
#pragma unroll
    for (int t = 0; t < 14; ++t) {
#pragma unroll
      for (int x = 0; x < 2; ++x) {
        u32x4 wf;
        if (t < 11) wf = W2R[t][x];
        else {
          const unsigned a = LR + (w2l ^ (64u * x)) + (t - 11) * 2048;
          wf = trfrag(a, a + 1024);
        }
        acc[x][0] = mfma_bf(wf, H[t][0], acc[x][0]);
        acc[x][1] = mfma_bf(wf, H[t][1], acc[x][1]);
      }
      if (t < 2) {
#pragma unroll
        for (int k = 0; k < 4; ++k)
          H[10 + 2 * t + (k >> 1)][k & 1] = __builtin_amdgcn_raw_buffer_load_b128(rws, hi + (k & 1) * 1024, (10 + 2 * t + (k >> 1)) * 2048, 16);
        SB();
      }
    }
#pragma unroll
    for (int k = 0; k < 8; ++k) {
      u32x4 v;
#pragma unroll
      for (int e = 0; e < 4; ++e) v[e] = pk_bf2(pr(acc[k >> 2][(k >> 1) & 1], 8 * (k & 1) + 2 * e));
      lds128w(LR + (unsigned)(k * 1024 + lane * 16), v);
    }
    SB();
    STAMP(19);
    const int rho = rgw & 1, kap = rgw >> 1;
    u32x4 I[15][2];
#pragma unroll
    for (int x = 0; x < 2; ++x) {
      const float d = (float)(64 * cg + 32 * x + l31), dl = d * (1.0f / 448.0f);
      const float tw = 2.0f * cosr(rev448(8.0f * d));
      const float k0 = (float)(112 * kap + 4 * h);
      const float a0 = rev448(k0 * d), a1 = rev448((k0 + 8.0f) * d);
      f32x2 f0[4], f1[4];
#pragma unroll
      for (int e = 0; e < 4; ++e) {
        const float b0 = __builtin_fmaf((float)e, dl, a0), b1 = __builtin_fmaf((float)e, dl, a1);
        f0[e >> 1][e & 1] = cosr(b0);
        f0[2 + (e >> 1)][e & 1] = sinr(-b0);
        f1[e >> 1][e & 1] = cosr(b1);
        f1[2 + (e >> 1)][e & 1] = sinr(-b1);
      }
#pragma unroll
      for (int i = 0; i < 15; ++i) {
#pragma unroll
        for (int e = 0; e < 4; ++e) {
          I[i][x][e] = pk_bf2(f0[e]);
          const f32x2 n = fma2(bc(tw), f1[e], -f0[e]);
          f0[e] = f1[e];
          f1[e] = n;
        }
        PIN(I[i][x]);
      }
    }
    u32x4 GC[2][2];
#pragma unroll
    for (int sh = 0; sh < 2; ++sh) {
      const float sf = (float)(32 * (32 * sh + l31) + s2) * (1.0f / 2048.0f);
#pragma unroll
      for (int t = 0; t < 2; ++t)
#pragma unroll
        for (int e = 0; e < 4; ++e) {
          const int p = 16 * rho + 8 * t + 4 * (e >> 1) + 2 * h + (e & 1);
          const float a = (float)p * sf;
          float sv = sinr(a);
          if (t == 0 && e == 0) sv = (p == 0) ? ((l31 & 1) ? -1.0f : 1.0f) : sv;
          GC[sh][t][e] = pk_bf(cosr(a), sv);
        }
      PIN(GC[sh][0]); PIN(GC[sh][1]);
    }
    unsigned ai = WS_A + (unsigned)(((s2 * 2 + rho) * 29 + 14 * kap) * 1024 + (l31 >> 3) * 256 + h * 128 + (l31 & 7) * 16);
    PIN(ai);
    STAMP(20);
    {
      const int gi = lane >> 2, wv = (gi & 7) == 7 ? ((kap == 1 && gi == 15) ? 7 : 6) : (gi & 7);
      poll(sws, WS_AF + (unsigned)(((4 * rho + (lane & 3)) * 4 + 2 * kap + (gi >> 3)) * 128 + wv * 8), tag);
    }
    STAMP(21);
    u32x4 A[15];
#pragma unroll
    for (int i = 0; i < 8; ++i) A[i] = __builtin_amdgcn_raw_buffer_load_b128(rws, ai, i * 1024, 16);
    SB();
    f32x16 T[2] = {};
#pragma unroll
    for (int i = 0; i < 15; ++i) {
      T[0] = mfma_bf(A[i], I[i][0], T[0]);
      T[1] = mfma_bf(A[i], I[i][1], T[1]);
      if (i < 3) {
#pragma unroll
        for (int k = 8 + 3 * i; k < 11 + 3 * i && k < 15; ++k)
          A[k] = __builtin_amdgcn_raw_buffer_load_b128(rws, (k == 14 && kap == 0) ? OOB : ai, k * 1024, 16);
        if (i == 2) {
          xres = __builtin_amdgcn_raw_buffer_load_b128(rx, oo, 0, 0);
          b2v = __builtin_amdgcn_raw_buffer_load_b128(rb2, (unsigned)(128 * cg + (tid & 7) * 16), 0, 0);
        }
        SB();
      }
      if (i >= 7) {
        const int k = i - 7;
        const u32x4 v = lds128(LR + (unsigned)(k * 1024 + lane * 16));
#pragma unroll
        for (int e = 0; e < 4; ++e) {
          acc[k >> 2][(k >> 1) & 1][8 * (k & 1) + 2 * e] = bf_lo(v[e]);
          acc[k >> 2][(k >> 1) & 1][8 * (k & 1) + 2 * e + 1] = bf_hi(v[e]);
        }
        SB();
      }
    }
#pragma unroll
    for (int x = 0; x < 2; ++x)
#pragma unroll
      for (int t = 0; t < 2; ++t) {
        u32x4 tf;
#pragma unroll
        for (int e = 0; e < 4; ++e) tf[e] = pk_bf2(pr(T[x], 8 * t + 2 * e));
        acc[x][0] = mfma_bf(tf, GC[0][t], acc[x][0]);
        acc[x][1] = mfma_bf(tf, GC[1][t], acc[x][1]);
      }
  } else {
    WAITVM(0);
    if (lane == 0) __builtin_amdgcn_raw_buffer_store_b64(tag, rws, hfo, 0, 16);
    STAMP(16);
#pragma unroll 1
    for (int n = 0; n < SPIN_CAP; ++n) {
      const unsigned dn = *(volatile LAS unsigned*)(LB + L_DONE + (lane & 7) * 4);
      if (__builtin_amdgcn_ballot_w64(dn == 0u) == 0) break;
    }
    asm volatile("" ::: "memory");
#pragma unroll
    for (int pc = 0; pc < 14; ++pc) w2dma(LQ + pc * 1024, pc);
    STAMP(17);
    poll(sws, hpo, tag);
    STAMP(18);
#pragma unroll
    for (int pc = 14; pc < 28; ++pc) w2dma(LR + (pc - 6) * 1024, pc);
    u32x4 H[14][2];
#pragma unroll
    for (int t = 0; t < 14; ++t)
#pragma unroll
      for (int sh = 0; sh < 2; ++sh) H[t][sh] = __builtin_amdgcn_raw_buffer_load_b128(rws, hi + sh * 1024, t * 2048, 16);
    xres = __builtin_amdgcn_raw_buffer_load_b128(rx, oo, 0, 0);
    b2v = __builtin_amdgcn_raw_buffer_load_b128(rb2, (unsigned)(128 * cg + (tid & 7) * 16), 0, 0);
    WAITVM(30);
    STAMP(19);
#pragma unroll
    for (int t = 0; t < 14; ++t)
#pragma unroll
      for (int x = 0; x < 2; ++x) {
        const unsigned a = (t < 7 ? LQ + t * 2048 : LR + (t - 3) * 2048) + (w2l ^ (64u * x));
        const u32x4 wf = trfrag(a, a + 1024);
        acc[x][0] = mfma_bf(wf, H[t][0], acc[x][0]);
        acc[x][1] = mfma_bf(wf, H[t][1], acc[x][1]);
      }
  }
  {
    const unsigned pb = (grp ? LQ : LR) + (unsigned)(l31 * 128 + h * 8);
#pragma unroll
    for (int x = 0; x < 2; ++x)
#pragma unroll
      for (int sh = 0; sh < 2; ++sh)
#pragma unroll
        for (int q = 0; q < 4; ++q) {
          const u32x2 v = {pk_bf2(pr(acc[x][sh], 4 * q)), pk_bf2(pr(acc[x][sh], 4 * q + 2))};
          *(LAS u32x2*)(pb + (unsigned)(sh * 4096 + (((4 * x + q) ^ (l31 & 7)) * 16))) = v;
        }
  }
  STAMP(22);
  LBAR();
  {
    const unsigned ra = LB + (unsigned)((tid >> 3) * 128 + (((tid & 7) ^ ((tid >> 3) & 7)) * 16));
    f32x2 r[4];
#pragma unroll
    for (int e = 0; e < 4; ++e) r[e] = f32x2{bf_lo(xres[e]), bf_hi(xres[e])} + f32x2{bf_lo(b2v[e]), bf_hi(b2v[e])};
#pragma unroll
    for (int k = 0; k < 8; ++k) {
      const u32x4 v = lds128(ra + (k < 4 ? L_C0 + k * 22528 : (k - 4) * 14336));
#pragma unroll
      for (int e = 0; e < 4; ++e) r[e] += f32x2{bf_lo(v[e]), bf_hi(v[e])};
    }
    const u32x4 o = {pk_bf2(r[0]), pk_bf2(r[1]), pk_bf2(r[2]), pk_bf2(r[3])};
    __builtin_amdgcn_raw_buffer_store_b128(o, rout, oo, 0, 16);
  }
  if (blockIdx.x == 0 && tid == 0) __builtin_amdgcn_raw_buffer_store_b64(tag, rws, WS_E, 0, 16);
  STAMP(23);
  STAMP_OUT();
}

extern "C" void kernel_launch(void* const* d_in, const int* in_sizes, int n_in, void* d_out, int out_size,
                              void* d_ws, size_t ws_size, hipStream_t stream) {
  fused_kernel<<<256, 512, 0, stream>>>(d_in[0], d_in[1], d_in[2], d_in[4], d_in[5], d_in[6], d_in[7], d_out, d_ws);
}
```

```cpp
#include <hip/hip_runtime.h>
#include <stdint.h>
#include <type_traits>
#pragma clang diagnostic ignored "-Wint-to-pointer-cast"

#define DEV __device__ __forceinline__
#define INL __attribute__((always_inline))
#if defined(__HIP_DEVICE_COMPILE__)
#define NOPK __attribute__((target("no-packed-fp32-ops")))
#else
#define NOPK
#endif
#define LAS __attribute__((address_space(3)))
typedef __attribute__((ext_vector_type(4))) short s16x4;
typedef __attribute__((ext_vector_type(8))) short s16x8;
typedef __attribute__((ext_vector_type(8))) __bf16 bf16x8;
typedef __attribute__((ext_vector_type(2))) __bf16 bf16x2;
typedef __attribute__((ext_vector_type(8))) _Float16 f16x8;
typedef __attribute__((ext_vector_type(2))) _Float16 f16x2;
typedef __attribute__((ext_vector_type(2))) float f32x2;
typedef __attribute__((ext_vector_type(4))) float f32x4;
typedef __attribute__((ext_vector_type(16))) float f32x16;
typedef __attribute__((ext_vector_type(2))) unsigned u32x2;
typedef __attribute__((ext_vector_type(4))) unsigned u32x4;
typedef __amdgpu_buffer_rsrc_t rsrc_t;

constexpr unsigned WS_E = 0;
constexpr unsigned WS_ZF = 4096;
constexpr unsigned WS_HF = WS_ZF + 224 * 512;
constexpr unsigned WS_AF = WS_HF + 224 * 1024;
constexpr unsigned WS_CS = WS_AF + 4096;
constexpr unsigned WS_Z = 393216;
constexpr unsigned WS_A = WS_Z + 29 * 16 * 4096;
constexpr unsigned WS_H = WS_A + 32 * 2 * 29 * 1024;
constexpr unsigned WS_END = WS_H + 32 * 112 * 2048;
constexpr unsigned OOB = 0x80000000u;
constexpr int SPIN_CAP = 1 << 16;
#define POLL_SLEEP 8
#define CODE_BYTES 31488

constexpr unsigned L_PROG = 0;
constexpr unsigned L_RS = 512;
constexpr unsigned L_XA = 1024;
constexpr unsigned L_C0 = 57344;
constexpr unsigned L_E = 114688;
constexpr unsigned L_GATH = L_E + 32768;
DEV constexpr unsigned xblk(int kc) { return kc < 4 ? 2048u + 16384u * kc : L_E + 16384u * (kc - 4); }

#define WAITBAR(n) asm volatile("s_waitcnt vmcnt(" #n ") lgkmcnt(0)\n\ts_barrier" ::: "memory")
#define LBAR() asm volatile("s_waitcnt lgkmcnt(0)\n\ts_barrier" ::: "memory")
#define PIN(x) asm volatile("" : "+v"(x))
#define SB() __builtin_amdgcn_sched_barrier(0)
#define STAMP(i)
#define STAMP_OUT()
#define WAITVM(n) asm volatile("s_waitcnt vmcnt(" #n ")" ::: "memory")

DEV u32x4 mk_srd(const void* p, unsigned bytes) {
  uint64_t a = (uint64_t)p;
  u32x4 r = {(unsigned)a, (unsigned)(a >> 32) & 0xffffu, bytes, 0x00020000u};
  return r;
}
DEV void dma16(unsigned lds, unsigned voff, u32x4 srd, unsigned soff) {
  asm volatile("s_mov_b32 m0, %0\n\ts_nop 0\n\tbuffer_load_dwordx4 %1, %2, %3 offen lds"
               :: "s"(lds), "v"(voff), "s"(srd), "s"(soff) : "memory");
}
DEV u32x4 lds128(unsigned a) { return *(LAS u32x4*)a; }
DEV void lds128w(unsigned a, u32x4 v) { *(LAS u32x4*)a = v; }
DEV u32x4 trfrag(unsigned a0, unsigned a1) {
  s16x4 lo = __builtin_amdgcn_ds_read_tr16_b64_v4i16((LAS s16x4*)a0);
  s16x4 hi = __builtin_amdgcn_ds_read_tr16_b64_v4i16((LAS s16x4*)a1);
  return __builtin_bit_cast(u32x4, __builtin_shufflevector(lo, hi, 0, 1, 2, 3, 4, 5, 6, 7));
}
DEV f32x16 mfma_bf(u32x4 a, u32x4 b, f32x16 c) {
  return __builtin_amdgcn_mfma_f32_32x32x16_bf16(__builtin_bit_cast(bf16x8, a), __builtin_bit_cast(bf16x8, b), c, 0, 0, 0);
}
DEV f32x16 mfma_h(u32x4 a, u32x4 b, f32x16 c) {
  return __builtin_amdgcn_mfma_f32_32x32x16_f16(__builtin_bit_cast(f16x8, a), __builtin_bit_cast(f16x8, b), c, 0, 0, 0);
}
DEV unsigned pk_bf(float lo, float hi) {
  f32x2 p = {lo, hi};
  return __builtin_bit_cast(unsigned, __builtin_convertvector(p, bf16x2));
}
DEV unsigned pk_h(float lo, float hi) {
  f32x2 p = {lo, hi};
  return __builtin_bit_cast(unsigned, __builtin_convertvector(p, f16x2));
}
DEV unsigned pk_bf2(f32x2 p) { return __builtin_bit_cast(unsigned, __builtin_convertvector(p, bf16x2)); }
DEV unsigned pk_h2(f32x2 p) { return __builtin_bit_cast(unsigned, __builtin_convertvector(p, f16x2)); }
DEV f32x2 pr(const f32x16& v, int r) { return f32x2{v[r], v[r + 1]}; }
DEV f32x2 fma2(f32x2 a, f32x2 b, f32x2 c) { return __builtin_elementwise_fma(a, b, c); }
DEV f32x2 bc(float a) { return f32x2{a, a}; }
DEV float bf_lo(unsigned v) { return __builtin_bit_cast(float, v << 16); }
DEV float bf_hi(unsigned v) { return __builtin_bit_cast(float, v & 0xffff0000u); }
DEV float bf16r(float x) { return bf_lo(pk_bf(x, 0.f)); }
DEV float dot2(unsigned a, unsigned b, float c) {
  return __builtin_amdgcn_fdot2_f32_bf16(__builtin_bit_cast(bf16x2, a), __builtin_bit_cast(bf16x2, b), c, false);
}
DEV float cosr(float rev) { return __builtin_amdgcn_cosf(rev); }
DEV float sinr(float rev) { return __builtin_amdgcn_sinf(rev); }
DEV float rev448(float n) {
  float q = __builtin_floorf(n * (1.0f / 448.0f));
  return __builtin_fmaf(q, -448.0f, n) * (1.0f / 448.0f);
}
DEV void poll(u32x4 srd, unsigned voff, u32x2 tag) {
#pragma unroll 1
  for (int n = 0; n < SPIN_CAP; ++n) {
    u32x2 v;
    asm volatile("buffer_load_dwordx2 %0, %1, %2, 0 offen sc1\n\ts_waitcnt vmcnt(0)"
                 : "=&v"(v) : "v"(voff), "s"(srd) : "memory");
    if (__builtin_amdgcn_ballot_w64(v[0] != tag[0] || v[1] != tag[1]) == 0) break;
    __builtin_amdgcn_s_sleep(POLL_SLEEP);
  }
}
DEV u32x2 mk_tag(u32x2 ev) {
  uint64_t e = ((uint64_t)__builtin_amdgcn_readfirstlane(ev[1]) << 32) | (unsigned)__builtin_amdgcn_readfirstlane(ev[0]);
  e = e * 6364136223846793005ull + 1442695040888963407ull;
  return u32x2{(unsigned)e, (unsigned)(e >> 32)};
}
template <int A, int B, class Fn> DEV void sfor(Fn&& f) {
  if constexpr (A < B) { f(std::integral_constant<int, A>{}); sfor<A + 1, B>(f); }
}
DEV int shx(int v, int m) { return __builtin_amdgcn_ds_bpermute((int)((__builtin_amdgcn_mbcnt_hi(~0u, __builtin_amdgcn_mbcnt_lo(~0u, 0u)) ^ m) << 2), v); }
DEV float xor_add(float v, int m) { return v + __builtin_bit_cast(float, shx(__builtin_bit_cast(int, v), m)); }
DEV int imin(int a, int b) { return a < b ? a : b; }

DEV void dma_x(unsigned lds, unsigned lit, unsigned v0, unsigned v1, u32x4 srd, unsigned s1, unsigned s2, unsigned s3, int o) {
  asm volatile("s_add_u32 m0, %0, %1\n\ts_nop 0\n\t"
               "buffer_load_dwordx4 %2, %4, 0 offen offset:%8 lds\n\t"
               "buffer_load_dwordx4 %3, %4, %5 offen offset:%8+1024 lds\n\t"
               "buffer_load_dwordx4 %2, %4, %6 offen offset:%8+2048 lds\n\t"
               "buffer_load_dwordx4 %3, %4, %7 offen offset:%8+3072 lds"
               :: "s"(lds), "n"(lit), "v"(v0), "v"(v1), "s"(srd), "s"(s1), "s"(s2), "s"(s3), "n"(o) : "memory", "scc");
}
template <int N> DEV void dma_w1(unsigned lds, unsigned lit, unsigned v, u32x4 srd, unsigned so, unsigned step) {
  unsigned t;
  asm volatile("s_add_u32 m0, %1, %2\n\ts_mov_b32 %0, %5\n\t"
               "buffer_load_dwordx4 %3, %4, %0 offen lds\n\ts_add_u32 %0, %0, %6\n\t"
               "buffer_load_dwordx4 %3, %4, %0 offen offset:1024 lds\n\t"
               ".if %7 > 2\n\ts_add_u32 %0, %0, %6\n\tbuffer_load_dwordx4 %3, %4, %0 offen offset:2048 lds\n\t.endif\n\t"
               ".if %7 > 3\n\ts_add_u32 %0, %0, %6\n\tbuffer_load_dwordx4 %3, %4, %0 offen offset:3072 lds\n\t.endif"
               : "=&s"(t) : "s"(lds), "n"(lit), "v"(v), "s"(srd), "n"(so), "s"(step), "n"(N) : "memory", "scc");
}
template <int N> DEV void dma_w2(unsigned lds, unsigned lit, unsigned v0, unsigned v1, u32x4 srd, unsigned so) {
  unsigned t;
  asm volatile("s_add_u32 m0, %1, %2\n\ts_mov_b32 %0, %6\n\t"
               "buffer_load_dwordx4 %3, %5, %0 offen lds\n\t"
               "buffer_load_dwordx4 %4, %5, %0 offen offset:1024 lds\n\t"
               ".if %7 > 1\n\ts_add_u32 %0, %0, 12288\n\t"
               "buffer_load_dwordx4 %3, %5, %0 offen offset:2048 lds\n\t"
               "buffer_load_dwordx4 %4, %5, %0 offen offset:3072 lds\n\t.endif"
               : "=&s"(t) : "s"(lds), "n"(lit), "v"(v0), "v"(v1), "s"(srd), "n"(so), "n"(N) : "memory", "scc");
}
DEV void touch(unsigned lds, unsigned voff, u32x4 srd) {
  asm volatile("s_mov_b32 m0, %0\n\ts_nop 0\n\tbuffer_load_dword %1, %2, 0 offen lds" :: "s"(lds), "v"(voff), "s"(srd) : "memory");
}
DEV u32x2 get_tag(const void* pws) {
  uint64_t e = *(const __attribute__((address_space(4))) uint64_t*)(uintptr_t)pws;
  e = e * 6364136223846793005ull + 1442695040888963407ull;
  asm volatile("" : "+s"(e));
  return u32x2{(unsigned)e, (unsigned)(e >> 32)};
}
DEV void wait_prog(unsigned a, unsigned need, unsigned mask) {
#pragma unroll 1
  for (int n = 0; n < SPIN_CAP; ++n) {
    const unsigned v = *(volatile LAS unsigned*)a;
    if (((unsigned)__builtin_amdgcn_ballot_w64(v < need) & mask) == 0) break;
  }
  asm volatile("" ::: "memory");
}
DEV f32x4 mfma4(unsigned a0, unsigned a1, unsigned b0, unsigned b1, f32x4 c) {
  return __builtin_amdgcn_mfma_f32_4x4x4bf16_1k(__builtin_bit_cast(s16x4, u32x2{a0, a1}), __builtin_bit_cast(s16x4, u32x2{b0, b1}), c, 0, 0, 0);
}
DEV float half_sum(float v) {
  const unsigned u = __builtin_bit_cast(unsigned, v);
  const auto r = __builtin_amdgcn_permlane32_swap(u, u, false, false);
  return __builtin_bit_cast(float, (unsigned)r[0]) + __builtin_bit_cast(float, (unsigned)r[1]);
}
DEV f16x2 h2(float a, float b) { return f16x2{(_Float16)a, (_Float16)b}; }
DEV f16x2 hfma(f16x2 a, f16x2 b, f16x2 c) { return __builtin_elementwise_fma(a, b, c); }

__global__ void NOPK __launch_bounds__(512, 2)
fused_kernel(const void* __restrict__ px, const void* __restrict__ pw1, void* __restrict__ pws,
             const void* __restrict__ pw2, const void* __restrict__ pwr, const void* __restrict__ pwi,
             const void* __restrict__ pb1, const void* __restrict__ pb2, void* __restrict__ pout) {
  __shared__ __attribute__((aligned(1024))) char smem[163840];
  const unsigned LB = (unsigned)(uintptr_t)(LAS char*)smem;
  const int tid = threadIdx.x;
  const int lane = tid & 63;
  const int w = __builtin_amdgcn_readfirstlane(tid >> 6);
  const int l31 = lane & 31, h = lane >> 5;
  const int xcd = blockIdx.x & 7, loc = blockIdx.x >> 3;
  if (w == 4)
    touch(LB + 256, (unsigned)(loc + 32 * lane) * 128u, mk_srd((const void*)__builtin_amdgcn_s_getpc(), CODE_BYTES));
  asm volatile("" :: "s"(px), "s"(pw1), "s"(pws), "s"(pw2));
  const u32x4 sws = mk_srd(pws, WS_END);
  const rsrc_t rws = __builtin_amdgcn_make_buffer_rsrc(pws, 0, WS_END, 0x00020000);
  const u32x4 sw2 = mk_srd(pw2, 448 * 1792 * 2);
  STAMP(0);

  if (loc >= 28) {
    const int pq = xcd, qt = loc - 28;
    const int ch = tid & 7, rw = tid >> 3;
    const rsrc_t rw1 = __builtin_amdgcn_make_buffer_rsrc((void*)pw1, 0, 448 * 1792 * 2, 0x00020000);
    const rsrc_t rb1 = __builtin_amdgcn_make_buffer_rsrc((void*)pb1, 0, 1792 * 2, 0x00020000);
    const rsrc_t rwr = __builtin_amdgcn_make_buffer_rsrc((void*)pwr, 0, 2048 * 225 * 2, 0x00020000);
    const rsrc_t rwi = __builtin_amdgcn_make_buffer_rsrc((void*)pwi, 0, 2048 * 225 * 2, 0x00020000);
    const int sl = 4 * (xcd >> 1) + qt;
    const int col0 = (sl < 7 ? 0 : 896 - 448) + 448 * (xcd & 1) + 64 * sl;
    u32x4 cw[7];
    {
      const unsigned o = (sl < 14) ? (unsigned)(rw * 3584 + col0 * 2 + ch * 16) : OOB;
#pragma unroll
      for (int i = 0; i < 7; ++i) cw[i] = __builtin_amdgcn_raw_buffer_load_b128(rw1, o, i * 64 * 3584, 0);
    }
    const unsigned b1v = __builtin_amdgcn_raw_buffer_load_b16(rb1, (unsigned)(col0 + lane) * 2u, 0, 0);
    SB();
    const u32x2 tag = get_tag(pws);
    {
      float c8[8];
#pragma unroll
      for (int e = 0; e < 8; ++e) {
        float t = 0.f;
#pragma unroll
        for (int i = 0; i < 7; ++i) t += (e & 1) ? bf_hi(cw[i][e >> 1]) : bf_lo(cw[i][e >> 1]);
        c8[e] = t + __builtin_bit_cast(float, __builtin_amdgcn_update_dpp(0, __builtin_bit_cast(int, t), 0x128, 15, 15, false));
      }
      const unsigned a = LB + 81920u + (unsigned)((tid >> 4) * 272 + ch * 32 + (lane & 8) * 2);
      *(LAS f32x4*)a = (lane & 8) ? f32x4{c8[4], c8[5], c8[6], c8[7]} : f32x4{c8[0], c8[1], c8[2], c8[3]};
    }
    if (w < 7) touch(LB + 98304, (unsigned)((112 * sl + 16 * w + (lane >> 2)) * 896 + (3 * (xcd & 1) + (lane & 3)) * 128), sw2);
    LBAR();
    if (w == 7) {
      float c0 = 0.f, c1 = 0.f;
#pragma unroll
      for (int k = 0; k < 32; k += 2) {
        c0 += *(LAS float*)(LB + 81920u + (unsigned)(k * 272 + lane * 4));
        c1 += *(LAS float*)(LB + 81920u + (unsigned)(k * 272 + 272 + lane * 4));
      }
      const float cs = c0 + c1, hi = bf16r(cs);
      const u32x4 gr = {pk_bf(hi, cs - hi), tag[0], b1v, tag[1]};
      __builtin_amdgcn_raw_buffer_store_b128(gr, rws, sl < 14 ? WS_CS + (unsigned)(col0 + lane) * 16u : OOB, 0, 16);
    }
    STAMP(1);
    u32x4 wv[8];
    unsigned wx[8];
#pragma unroll
    for (int i = 0; i < 4; ++i) {
      const int idx = rw + 64 * (i & 1), p = 4 * pq + (idx & 3), k2 = idx >> 2;
      const int k = (i >> 1) ? (p ? ((2048 - p + 64 * k2) & 2047) : (32 + 64 * k2)) : (p + 64 * k2);
      const unsigned o = (unsigned)(k * 225 + 56 * qt + 8 * ch) * 2u;
      const unsigned o4 = (ch < 7) ? o : OOB, o1 = (ch == 7 && qt == 3) ? o : OOB;
      wv[i] = __builtin_amdgcn_raw_buffer_load_b128(rwr, o4, 0, 0);
      wv[4 + i] = __builtin_amdgcn_raw_buffer_load_b128(rwi, o4, 0, 0);
      wx[i] = __builtin_amdgcn_raw_buffer_load_b16(rwr, o1, 0, 0);
      wx[4 + i] = __builtin_amdgcn_raw_buffer_load_b16(rwi, o1, 0, 0);
    }
    SB();
    const bool active = (w < 7) || (qt == 3);
    const int g = 7 * qt + w;
    const int kdl = lane & 7, pl = (lane >> 3) & 3;
    const int p = 4 * pq + pl, kd = 8 * g + kdl;
    u32x4 FR[4], FI[4], IR[4], II[4];
#pragma unroll
    for (int t = 0; t < 4; ++t) {
      float c[4], sn[4];
#pragma unroll
      for (int e = 0; e < 4; ++e) {
        const float a = (float)(l31 * (8 * t + 4 * h + e)) * (1.0f / 32.0f);
        c[e] = cosr(a);
        sn[e] = sinr(a);
        FR[t][e] = pk_h(c[e], sn[e]);
        FI[t][e] = pk_h(-sn[e], c[e]);
      }
      IR[t] = u32x4{pk_bf(c[0], c[1]), pk_bf(c[2], c[3]), pk_bf(-sn[0], -sn[1]), pk_bf(-sn[2], -sn[3])};
      II[t] = u32x4{pk_bf(sn[0], sn[1]), pk_bf(sn[2], sn[3]), pk_bf(c[0], c[1]), pk_bf(c[2], c[3])};
      PIN(FR[t]); PIN(FI[t]); PIN(IR[t]); PIN(II[t]);
    }
#pragma unroll
    for (int i = 0; i < 8; ++i) {
      wv[i][0] |= wx[i];
      lds128w(LB + (unsigned)(((i >> 1) * 128 + rw + 64 * (i & 1)) * 144 + ch * 16), wv[i]);
    }
    LBAR();
    if (active) {
      f32x2 wpr[8], wpi[8], wmr[8], wmi[8];
      const float wsc = ((kd == 0 || kd == 224) ? 1.0f : 2.0f) / (2048.0f * 448.0f);
      const unsigned wa = LB + (unsigned)((16 * h + pl) * 144 + (8 * w + kdl) * 2);
#pragma unroll
      for (int r = 0; r < 16; ++r) {
        const unsigned o = (unsigned)(((r & 3) + 8 * (r >> 2)) * 4 * 144);
        wpr[r >> 1][r & 1] = bf_lo(*(LAS unsigned short*)(wa + o));
        wmr[r >> 1][r & 1] = bf_lo(*(LAS unsigned short*)(wa + o + 128 * 144));
        wpi[r >> 1][r & 1] = bf_lo(*(LAS unsigned short*)(wa + o + 256 * 144));
        wmi[r >> 1][r & 1] = bf_lo(*(LAS unsigned short*)(wa + o + 384 * 144));
      }
      float ff = pq ? 1.0f : 0.0f;
      float cph = pq ? 1.0f : cosr((float)l31 * (1.0f / 64.0f));
      float sph = pq ? 0.0f : sinr((float)l31 * (1.0f / 64.0f));
      PIN(ff); PIN(cph); PIN(sph);
#pragma unroll
      for (int r = 0; r < 8; ++r) {
        wpr[r] *= bc(wsc); wpi[r] *= bc(wsc); wmr[r] *= bc(wsc); wmi[r] *= bc(wsc);
        PIN(wpr[r]); PIN(wpi[r]); PIN(wmr[r]); PIN(wmi[r]);
      }
      unsigned zo = (kd > 224) ? OOB : WS_Z + (unsigned)((g * 16 + 2 * h) * 4096 + p * 128 + kdl * 16);
      unsigned ao = WS_A + (unsigned)((((lane >> 4) * 2 + (pq >> 2)) * 29 + g) * 1024 + (pq & 3) * 256 + (lane & 15) * 16);
      unsigned tw = LB + 90112u + (unsigned)(w * 8192 + l31 * 256);
      unsigned tr = LB + 90112u + (unsigned)(w * 8192 + (lane >> 4) * 256);
      PIN(zo); PIN(ao); PIN(tw); PIN(tr);
      STAMP(2);
      const int jz = (g == 28) ? 0 : (g >> 1);
      poll(sws, WS_ZF + (unsigned)(((lane & 15) * 14 + jz) * 512 + (pq >> 1) * 128), tag);
      STAMP(3);

      u32x4 z[8];
#pragma unroll
      for (int i = 0; i < 8; ++i)
        z[i] = __builtin_amdgcn_raw_buffer_load_b128(rws, zo, (4 * (i >> 1) + (i & 1)) * 4096, 16);
      SB();
      f32x16 xpr = {}, xpi = {}, xmr = {}, xmi = {};
#pragma unroll
      for (int t = 0; t < 4; ++t) {
        const u32x4 zp = {z[2 * t][0], z[2 * t][1], z[2 * t + 1][0], z[2 * t + 1][1]};
        xpr = mfma_h(FR[t], zp, xpr);
        xpi = mfma_h(FI[t], zp, xpi);
      }
#pragma unroll
      for (int t = 0; t < 4; ++t) {
        const u32x4 zm = {z[2 * t][2], z[2 * t][3], z[2 * t + 1][2], z[2 * t + 1][3]};
        xmr = mfma_h(FR[t], zm, xmr);
        xmi = mfma_h(FI[t], zm, xmi);
      }
      STAMP(4);
      f32x16 ypr = {}, ypi = {}, ymr = {}, ymi = {};
#pragma unroll
      for (int t = 0; t < 4; ++t) {
        u32x4 pp;
#pragma unroll
        for (int e = 0; e < 2; ++e) {
          const int r = 4 * t + 2 * e, q = 2 * t + e;
          pp[e] = pk_bf2(fma2(pr(xpr, r), wpr[q], -(pr(xpi, r) * wpi[q])));
          pp[2 + e] = pk_bf2(fma2(pr(xpr, r), wpi[q], pr(xpi, r) * wpr[q]));
        }
        ypr = mfma_bf(pp, IR[t], ypr);
        ypi = mfma_bf(pp, II[t], ypi);
      }
#pragma unroll
      for (int t = 0; t < 4; ++t) {
        u32x4 pm;
#pragma unroll
        for (int e = 0; e < 2; ++e) {
          const int r = 4 * t + 2 * e, q = 2 * t + e;
          pm[e] = pk_bf2(fma2(pr(xmr, r), wmr[q], -(pr(xmi, r) * wmi[q])));
          pm[2 + e] = pk_bf2(fma2(pr(xmr, r), wmi[q], pr(xmi, r) * wmr[q]));
        }
        ymr = mfma_bf(pm, IR[t], ymr);
        ymi = mfma_bf(pm, II[t], ymi);
      }
      STAMP(5);
#pragma unroll
      for (int q = 0; q < 4; ++q) {
        u32x4 uc, us;
#pragma unroll
        for (int e = 0; e < 2; ++e) {
          const int r = 4 * q + 2 * e;
          f32x2 acr, aci, asr, asi;
          if (q == 0) {
            acr = fma2(bc(ff), pr(ymr, r), pr(ypr, r));
            aci = fma2(bc(ff), pr(ymi, r), pr(ypi, r));
            asr = fma2(bc(cph), pr(ymi, r), fma2(bc(sph), pr(ymr, r), -(bc(ff) * pr(ypi, r))));
            asi = fma2(bc(sph), pr(ymi, r), fma2(bc(ff), pr(ypr, r), -(bc(cph) * pr(ymr, r))));
          } else {
            acr = pr(ypr, r) + pr(ymr, r);
            aci = pr(ypi, r) + pr(ymi, r);
            asr = pr(ymi, r) - pr(ypi, r);
            asi = pr(ypr, r) - pr(ymr, r);
          }
          uc[e] = pk_bf2(acr); uc[2 + e] = pk_bf2(aci);
          us[e] = pk_bf2(asr); us[2 + e] = pk_bf2(asi);
        }
        lds128w(tw + (unsigned)(((8 * h + 2 * q) ^ (l31 & 7)) * 16), uc);
        lds128w(tw + (unsigned)(((8 * h + 2 * q + 1) ^ (l31 & 7)) * 16), us);
      }
#pragma unroll
      for (int i = 0; i < 8; ++i) {
        const u32x4 v = lds128(tr + (unsigned)(i * 1024 + (((lane & 15) ^ ((4 * i + (lane >> 4)) & 7)) * 16)));
        __builtin_amdgcn_raw_buffer_store_b128(v, rws, ao + i * 8 * 29 * 1024, 0, 16);
      }
      STAMP(6);
    }
    WAITBAR(0);
    if (tid == 0) __builtin_amdgcn_raw_buffer_store_b64(tag, rws, WS_AF + (unsigned)((pq * 4 + qt) * 128), 0, 16);
    STAMP(7);
    STAMP_OUT();
    return;
  }

  const int q4 = loc / 7, j7 = loc % 7;
  const int rg = 4 * (xcd >> 1) + q4, j = 7 * (xcd & 1) + j7;
  const int s2 = 2 * rg + (j & 1), cg = j >> 1;
  const int rgw = w & 3, grp = w >> 2;
  const u32x4 sw1 = mk_srd(pw1, 448 * 1792 * 2);
  const unsigned swz16 = (unsigned)(((lane & 7) ^ ((lane >> 2) & 4)) * 16);
  const unsigned vw1 = (unsigned)((lane >> 3) * 3584 + 128 * j) + swz16;
  const unsigned vw2 = (unsigned)(((lane >> 3) + 224 * rgw) * 896 + 128 * cg) + swz16, vw2b = vw2 + 6144u;
  const unsigned bE = LB + L_E + rgw * 4096, bC = LB + L_C0 + rgw * 14336, bD = LB + 2048 + rgw * 12288;
  const unsigned pa = LB + L_PROG + (lane & 7) * 4;
  auto prog = [&](unsigned v) INL {
    asm volatile("" ::: "memory");
    *(volatile LAS unsigned*)(LB + L_PROG + w * 4) = v;
    asm volatile("" ::: "memory");
  };
  const unsigned cso = WS_CS + (unsigned)(896 * grp + 64 * j + l31) * 16u;
  unsigned stp = 28672 - 1024;
  asm volatile("" : "+s"(stp));

  u32x4 X[28];
  const unsigned xr = LB + (unsigned)(32 * rgw + l31) * 128u;
  const unsigned xs = (unsigned)(h ^ ((l31 >> 1) & 7));
  auto xread = [&](int u) INL { X[u] = lds128(xr + ((xs ^ (2 * (u & 3))) * 16) + xblk(u >> 2)); };
  u32x2 tag;
  u32x4 cg0, cg1, xaug;
  float sig, rstd;
#define CS_LOAD(tail) asm volatile("buffer_load_dwordx4 %0, %2, %3, 0 offen sc1\n\tbuffer_load_dwordx4 %1, %2, %3, 0 offen offset:512 sc1" tail \
                                   : "=&v"(cg0), "=&v"(cg1) : "v"(cso), "s"(sws) : "memory")

  if (grp == 0) {
    const u32x4 sx = mk_srd(px, 2048 * 448 * 2);
    if (rgw < 2) touch(LB + 256, (unsigned)((32 * lane + 2 * rg + rgw) * 896 + 128 * j7), sx);
    else touch(LB + 256, lane < 56 ? (unsigned)((112 * q4 + 56 * (rgw - 2) + lane) * 3584 + 128 * j) : OOB, sw1);
    const unsigned vx = (unsigned)((32 * (32 * (rgw & 1) + (lane >> 3)) + 2 * rg + (rgw >> 1)) * 896 + (((lane & 7) ^ (lane >> 4)) * 16));
    unsigned x1 = 229376 - 1024, x2 = 2 * (229376 - 1024), x3 = 3 * (229376 - 1024);
    asm volatile("" : "+s"(x1), "+s"(x2), "+s"(x3));
    const unsigned lx = LB + rgw * 4096;
    auto xdma = [&](auto kc) INL { dma_x(lx, xblk(kc) - kc * 128, vx, vx ^ 64u, sx, x1, x2, x3, kc * 128); };
    sfor<0, 4>(xdma);
    prog(0);
    const int pc0 = imin(10 + 12 * rgw, 44);
    const unsigned vwa = vw1 + (unsigned)pc0 * 28672u, lwa = LB + L_C0 + pc0 * 1024;
    tag = get_tag(pws);
    STAMP(1);
    f32x4 sa = {}, sb = {}, qa = {}, qb = {};
    sfor<0, 7>([&](auto ic) INL {
      constexpr int i = ic;
      if constexpr (i == 3 || i == 6) WAITBAR(12); else WAITVM(12);
      if constexpr (i == 0) STAMP(2);
      if constexpr (i == 3) STAMP(3);
      if constexpr (i == 6) STAMP(4);
#pragma unroll
      for (int u = 4 * i; u < 4 * i + 4; ++u) xread(u);
      SB();
      if constexpr (i < 3) xdma(std::integral_constant<int, i + 4>{});
      else if constexpr (i < 6) dma_w1<4>(lwa, (i - 3) * 4096, vwa, sw1, (i - 3) * 4 * 28672, stp);
      else {
        const int pc = imin(3 * rgw, 7);
        dma_w1<3>(LB + L_C0 + pc * 1024, 0, vw1 + (unsigned)pc * 28672u, sw1, 0, stp);
        CS_LOAD("");
      }
#pragma unroll
      for (int u = 4 * i; u < 4 * i + 4; ++u) {
        sa = mfma4(0x3f803f80u, 0x3f803f80u, X[u][0], X[u][1], sa);
        qa = mfma4(X[u][0], X[u][1], X[u][0], X[u][1], qa);
        sb = mfma4(0x3f803f80u, 0x3f803f80u, X[u][2], X[u][3], sb);
        qb = mfma4(X[u][2], X[u][3], X[u][2], X[u][3], qb);
      }
      SB();
    });
    qa += qb;
    const float q01 = (lane & 1) ? qa[1] : qa[0], q23 = (lane & 1) ? qa[3] : qa[2];
    const float mean = half_sum(sa[0] + sb[0]) * (1.0f / 448.0f);
    const float var = __builtin_fmaxf(__builtin_fmaf(-mean, mean, half_sum((lane & 2) ? q23 : q01) * (1.0f / 448.0f)), 0.0f);
    sig = bf16r(__builtin_amdgcn_sqrtf(bf16r(bf16r(var) + 1e-5f)));
    rstd = __builtin_amdgcn_rcpf(sig);
    xaug = u32x4{pk_bf(-mean, -mean), pk_bf(sig, 0.f), 0u, 0u};
    *(LAS float*)(LB + L_RS + (unsigned)(32 * rgw + l31) * 4u) = rstd;
    *(LAS u32x2*)(LB + L_XA + (unsigned)(32 * rgw + l31) * 8u) = u32x2{xaug[0], xaug[1]};
    prog(1);
    STAMP(5);
    WAITVM(5);
    prog(2);
    STAMP(6);
    wait_prog(pa, 2, 0x0f);
    __builtin_amdgcn_s_setprio(2);
  } else {
    prog(0);
    u32x4 F[28];
    {
      const bool sp = (j == 0) && (l31 == 16);
      const float kdf = sp ? 224.0f : (float)(16 * j + (l31 & 15));
      const float ph = ((l31 >> 4) && !sp) ? 0.25f : 0.0f;
      const float tw = 2.0f * cosr(rev448(16.0f * kdf));
      const float d0 = (float)(8 * h), dl = kdf * (1.0f / 448.0f);
      const float a0 = rev448(d0 * kdf) + ph, a1 = rev448((d0 + 16.0f) * kdf) + ph;
      float f0[8], f1[8];
#pragma unroll
      for (int e = 0; e < 8; ++e) {
        f0[e] = cosr(__builtin_fmaf((float)e, dl, a0));
        f1[e] = cosr(__builtin_fmaf((float)e, dl, a1));
      }
#pragma unroll
      for (int i = 0; i < 28; ++i) {
#pragma unroll
        for (int e = 0; e < 4; ++e) F[i][e] = pk_bf(f0[2 * e], f0[2 * e + 1]);
#pragma unroll
        for (int e = 0; e < 8; ++e) {
          const float n = __builtin_fmaf(tw, f1[e], -f0[e]);
          f0[e] = f1[e];
          f1[e] = n;
        }
        PIN(F[i]);
      }
    }
    STAMP(1);
    u32x4 GA[2][2];
    {
      const int hh = (l31 >> 2) & 1, reg = (l31 & 3) + 4 * (l31 >> 3);
      const int s2a = 2 * rg + (rgw >> 1);
#pragma unroll
      for (int mt = 0; mt < 2; ++mt) {
        const int p = 16 * mt + 8 * hh + (reg >> 1), cs = reg & 1;
        const bool sp = (p == 0) && cs;
        const float ca = sp ? 0.5f : (float)p * (1.0f / 64.0f);
        const float cb = __builtin_fmaf(ca, (float)(32 * (rgw & 1) + 4 * h), sp ? 0.0f : (float)(p * s2a) * (1.0f / 2048.0f) - 0.25f * (float)cs);
        const float tw = 2.0f * cosr(8.0f * ca);
        float g[4][4];
#pragma unroll
        for (int e = 0; e < 4; ++e) {
          g[0][e] = cosr(__builtin_fmaf(ca, (float)e, cb));
          g[1][e] = cosr(__builtin_fmaf(ca, (float)(e + 8), cb));
          g[2][e] = __builtin_fmaf(tw, g[1][e], -g[0][e]);
          g[3][e] = __builtin_fmaf(tw, g[2][e], -g[1][e]);
        }
#pragma unroll
        for (int e = 0; e < 8; ++e) GA[mt][e >> 2][e & 3] = pk_h(g[e >> 1][2 * (e & 1)], g[e >> 1][2 * (e & 1) + 1]);
        PIN(GA[mt][0]); PIN(GA[mt][1]);
      }
    }
    const int p = (tid & 255) >> 3, kdl = tid & 7;
    const float ff = p ? 1.0f : 0.0f;
    f16x2 k1 = h2(1.0f, -ff), k2 = h2(ff, 1.0f), k3[2], k4[2], k5[2];
#pragma unroll
    for (int sg = 0; sg < 2; ++sg) {
      const float ang = p ? 0.0f : (float)(2 * rg + sg) * (1.0f / 64.0f);
      const float c = cosr(ang), sn = sinr(ang);
      k3[sg] = h2(ff, c); k4[sg] = h2(-c, ff); k5[sg] = h2(sn, 0.0f);
      PIN(k3[sg]); PIN(k4[sg]); PIN(k5[sg]);
    }
    PIN(k1); PIN(k2);
    tag = get_tag(pws);
    STAMP(2);
    LBAR();
    STAMP(3);
    f32x16 U = {};
#pragma unroll
    for (int u = 0; u < 16; ++u) xread(u);
    SB();
#pragma unroll
    for (int u = 0; u < 16; ++u) U = mfma_bf(X[u], F[u], U);
    LBAR();
    __builtin_amdgcn_s_setprio(3);
    STAMP(4);
#pragma unroll
    for (int u = 16; u < 28; ++u) xread(u);
    SB();
    const unsigned vwc = vw1 + 1792u + (unsigned)rgw * (14u * 28672u), lwc = LB + rgw * 14336;
    sfor<0, 3>([&](auto ic) INL {
      constexpr int i = ic;
      dma_w1<4>(lwc, 2048 + i * 4096, vwc, sw1, (2 + 4 * i) * 28672, stp);
#pragma unroll
      for (int u = 16 + 4 * i; u < 20 + 4 * i; ++u) U = mfma_bf(X[u], F[u], U);
      SB();
    });
    asm volatile("s_waitcnt lgkmcnt(0)" ::: "memory");
    dma_w1<2>(rgw ? lwc : LB + L_E, 0, vwc, sw1, 0, stp);
    CS_LOAD("");
    STAMP(5);
    wait_prog(pa, 1, 1u << rgw);
    STAMP(6);
    {
      const u32x2 xa = *(LAS u32x2*)(LB + L_XA + (unsigned)(32 * rgw + l31) * 8u);
      rstd = *(LAS float*)(LB + L_RS + (unsigned)(32 * rgw + l31) * 4u);
      xaug = u32x4{xa[0], xa[1], 0u, 0u};
      sig = bf_lo(xa[1]);
    }
    const u32x4 faug = {(j == 0 && lane == 0) ? 0x43e0u : 0u, 0u, 0u, 0u};
    U = mfma_bf(xaug, faug, U);
    u32x4 ub[2];
#pragma unroll
    for (int q = 0; q < 4; ++q) {
      const f32x4 rr = *(LAS f32x4*)(LB + L_RS + (unsigned)(rgw * 32 + 8 * q + 4 * h) * 4u);
      ub[q >> 1][2 * (q & 1)] = pk_h(U[4 * q] * rr[0], U[4 * q + 1] * rr[1]);
      ub[q >> 1][2 * (q & 1) + 1] = pk_h(U[4 * q + 2] * rr[2], U[4 * q + 3] * rr[3]);
    }
    f32x16 za[2] = {};
#pragma unroll
    for (int t = 0; t < 2; ++t)
#pragma unroll
      for (int mt = 0; mt < 2; ++mt) za[mt] = mfma_h(GA[mt][t], ub[t], za[mt]);
#pragma unroll
    for (int mt = 0; mt < 2; ++mt)
#pragma unroll
      for (int q = 0; q < 2; ++q) {
        u32x4 v;
#pragma unroll
        for (int e = 0; e < 4; ++e) v[e] = pk_h2(pr(za[mt], 8 * q + 2 * e));
        lds128w(LB + L_GATH + (unsigned)(rgw * 4096 + l31 * 128 + (((4 * mt + 2 * h + q) ^ (l31 & 7)) * 16)), v);
      }
    prog(1);
    STAMP(7);
    wait_prog(pa, 1, 0xf0);
    STAMP(8);
    const unsigned ga = LB + L_GATH + (unsigned)(kdl * 128 + (((p >> 2) ^ kdl) * 16) + (p & 3) * 4);
    const bool sp = (j == 0) && (kdl == 0);
    u32x4 zx;
#pragma unroll
    for (int gl = 0; gl < 2; ++gl) {
      u32x4 z;
#pragma unroll
      for (int sg = 0; sg < 2; ++sg) {
        const unsigned a = ga + (unsigned)(2 * sg * 4096 + gl * 1024);
        const f16x2 t0 = *(LAS f16x2*)a + *(LAS f16x2*)(a + 4096);
        f16x2 t1 = *(LAS f16x2*)(a + 2048) + *(LAS f16x2*)(a + 6144);
        if (gl == 0) {
          zx[sg] = __builtin_bit_cast(unsigned, t1 * k1);
          zx[2 + sg] = __builtin_bit_cast(unsigned, hfma(t1.yy, k5[sg], t1 * k3[sg]));
          t1 = sp ? f16x2{} : t1;
        }
        z[sg] = __builtin_bit_cast(unsigned, hfma(t0, k1, t1.yx * k2));
        z[2 + sg] = __builtin_bit_cast(unsigned, hfma(t1.yy, k5[sg].yx, hfma(t0.yy, k5[sg], hfma(t0, k3[sg], t1.yx * k4[sg]))));
      }
      __builtin_amdgcn_raw_buffer_store_b128(z, rws, WS_Z + (unsigned)(((2 * j + gl) * 16 + rg) * 4096 + (tid & 255) * 16), 0, 16);
    }
    __builtin_amdgcn_raw_buffer_store_b128(zx, rws, sp ? WS_Z + (unsigned)((28 * 16 + rg) * 4096 + p * 128) : OOB, 0, 16);
    SB();
    STAMP(9);
    dma_w2<1>(bE, 2048, vw2, vw2b, sw2, 0);
    dma_w2<2>(bE, 16384, vw2, vw2b, sw2, 14336);
    WAITVM(11);
    prog(2);
    STAMP(10);
    wait_prog(pa, 2, 0xf0);
    dma_w2<2>(bE, 32768, vw2, vw2b, sw2, 3 * 14336);
    __builtin_amdgcn_s_setprio(1);
  }
  STAMP(11);

  const int tq = (lane >> 2) & 3, tp = lane & 3, tg = (lane >> 4) & 1;
  const unsigned w1l = (unsigned)((8 * h + tq) * 128 + 64 * (tq >> 1) + 32 * tg + 8 * tp);
  const unsigned w1b = LB + (grp ? 0u : L_C0) + w1l, w1z = LB + (grp ? L_E : L_C0) + w1l;
#define SEQ_U(i) (((i) % 28 + 5) % 28)
  auto w1frag = [&](int i) INL {
    const unsigned a = ((SEQ_U(i) ? w1b : w1z) ^ (64u * (i / 28))) + 2048u * SEQ_U(i);
    return trfrag(a, a + 512);
  };
  const float gc1 = -2.302208198f * rstd, gc2 = gc1 * 0.044715f * rstd * rstd;
  const unsigned ho = WS_H + (unsigned)((((2 * rg + (rgw >> 1)) * 112 + 56 * grp + 4 * j) * 2 + (rgw & 1)) * 1024 + lane * 16);
  auto gelu_a = [&](float u) INL { return __builtin_fmaf(u * u, gc2, gc1) * u; };
  auto gelu_b = [&](float a) INL { return __builtin_fmaf(__builtin_amdgcn_exp2f(a), sig, sig); };
  f32x16 h0 = {}, h1 = {};
  {
    constexpr int PD = 5;
    u32x4 wf[56];
#pragma unroll
    for (int i = 0; i < PD; ++i) wf[i] = w1frag(i);
    u32x4 ho0[2];
    float g0, g1;
    sfor<0, 56>([&](auto ic) INL {
      constexpr int i = ic;
      if constexpr (i == 8) {
        if (grp == 0) {
          WAITVM(2);
          prog(3);
          STAMP(7);
        } else {
          WAITVM(10);
          if (lane == 0) __builtin_amdgcn_raw_buffer_store_b64(tag, rws, WS_ZF + (unsigned)((rg * 14 + j) * 512 + rgw * 128), 0, 16);
        }
      }
      if constexpr (i == 23 - PD) {
        if (grp == 0) { wait_prog(pa, 3, 0x0f); STAMP(8); }
      }
      if constexpr (i + PD < 56) wf[i + PD] = w1frag(i + PD);
      if constexpr (i < 28) h0 = mfma_bf(wf[i], X[SEQ_U(i)], h0);
      else h1 = mfma_bf(wf[i], X[SEQ_U(i)], h1);
      if constexpr (i == 25) {
        if (grp == 0) WAITVM(0);
        asm volatile("" : "+v"(cg0), "+v"(cg1));
#pragma unroll 1
        for (int n = 0; n < SPIN_CAP; ++n) {
          if (__builtin_amdgcn_ballot_w64(cg0[1] != tag[0] || cg0[3] != tag[1] || cg1[1] != tag[0] || cg1[3] != tag[1]) == 0) break;
          CS_LOAD("\n\ts_waitcnt vmcnt(0)");
        }
        cg0 = u32x4{h ? 0u : cg0[0], h ? 0u : cg0[2], 0u, 0u};
        cg1 = u32x4{h ? 0u : cg1[0], h ? 0u : cg1[2], 0u, 0u};
        if (grp == 0) STAMP(9);
      }
      if constexpr (i == 27) {
        h0 = mfma_bf(cg0, xaug, h0);
        h1 = mfma_bf(cg1, xaug, h1);
      }
      if constexpr (i == 40) { if (grp == 0) STAMP(10); }
      if constexpr (i == 36) {
        if (grp == 1) {
          WAITVM(1);
          prog(3);
        }
      }
      if constexpr (i >= 30 && i < 54) {
        constexpr int e = (i - 30) / 3, st = (i - 30) % 3;
        if constexpr (st == 0) { g0 = gelu_a(h0[2 * e]); g1 = gelu_a(h0[2 * e + 1]); PIN(g0); PIN(g1); }
        if constexpr (st == 1) { g0 = gelu_b(g0); g1 = gelu_b(g1); PIN(g0); PIN(g1); }
        if constexpr (st == 2) {
          unsigned o = pk_bf(h0[2 * e] * __builtin_amdgcn_rcpf(g0), h0[2 * e + 1] * __builtin_amdgcn_rcpf(g1));
          PIN(o);
          ho0[e >> 2][e & 3] = o;
          if constexpr ((e & 3) == 3) __builtin_amdgcn_raw_buffer_store_b128(ho0[e >> 2], rws, ho + (e >> 2) * 2048, 0, 16);
        }
      }
      SB();
    });
  }
  prog(4);
  __builtin_amdgcn_s_setprio(0);
  STAMP(12);
#pragma unroll
  for (int t = 0; t < 2; ++t) {
    u32x4 o;
#pragma unroll
    for (int e = 0; e < 4; ++e) {
      const float u0 = h1[8 * t + 2 * e], u1 = h1[8 * t + 2 * e + 1];
      o[e] = pk_bf(u0 * __builtin_amdgcn_rcpf(gelu_b(gelu_a(u0))), u1 * __builtin_amdgcn_rcpf(gelu_b(gelu_a(u1))));
    }
    __builtin_amdgcn_raw_buffer_store_b128(o, rws, ho + 4096 + t * 2048, 0, 16);
  }
  SB();
  STAMP(13);
  wait_prog(pa, 4, grp ? 0xf0u : 0x0fu);
  STAMP(14);

#define W2O(t) ((56 * ((t) / 14) + (t) % 14) * 14336)
  const rsrc_t rx = __builtin_amdgcn_make_buffer_rsrc((void*)px, 0, 2048 * 448 * 2, 0x00020000);
  const rsrc_t rb2 = __builtin_amdgcn_make_buffer_rsrc((void*)pb2, 0, 448 * 2, 0x00020000);
  const rsrc_t rout = __builtin_amdgcn_make_buffer_rsrc(pout, 0, 2048 * 448 * 2, 0x00020000);
  const unsigned oo = (unsigned)((32 * (tid >> 3) + s2) * 896 + 128 * cg + (tid & 7) * 16);
  if (grp == 0) {
    dma_w2<2>(bC, 0, vw2, vw2b, sw2, W2O(5));
    dma_w2<2>(bC, 4096, vw2, vw2b, sw2, W2O(7));
    dma_w2<2>(bC, 8192, vw2, vw2b, sw2, W2O(9));
    dma_w2<1>(bC, 12288, vw2, vw2b, sw2, W2O(11));
  } else {
    dma_w2<2>(bD, 0, vw2, vw2b, sw2, W2O(12));
    dma_w2<2>(bD, 4096, vw2, vw2b, sw2, W2O(14));
    dma_w2<2>(bD, 8192, vw2, vw2b, sw2, W2O(16));
    dma_w2<1>(bE, 0, vw2, vw2b, sw2, W2O(18));
  }
  STAMP(15);
  WAITVM(14);
  if (lane == 0) __builtin_amdgcn_raw_buffer_store_b64(tag, rws, WS_HF + (unsigned)((rg * 14 + j) * 1024 + w * 128), 0, 16);
  STAMP(16);
  u32x4 xres, b2v;
  f32x16 acc[2][2] = {};
  if (grp == 0) {
    const unsigned hpo = WS_HF + (unsigned)((rg * 14 + ((14 * rgw) >> 2) + ((lane >> 1) & 3)) * 1024 + (2 * (s2 & 1) + (lane & 1)) * 128);
    const unsigned hi = WS_H + (unsigned)((s2 * 112 + 14 * rgw) * 2048 + lane * 16);
    const unsigned w2l = (unsigned)((4 * h + tq) * 128 + 64 * (tq >> 1) + 32 * tg + 8 * tp);
    auto slot_b = [&](int t) INL { const int r = t < 19 ? t : t - 19; return r < 5 || r == 18 ? bE : r < 12 ? bC : bD; };
#define SLOT_R(t) ((t) < 19 ? (t) : (t) - 19)
#define SLOT_O(t) (SLOT_R(t) == 0 ? 2048 : SLOT_R(t) < 3 ? 14336 + 2048 * SLOT_R(t) : SLOT_R(t) < 5 ? 26624 + 2048 * SLOT_R(t) : \
                   SLOT_R(t) < 12 ? 2048 * (SLOT_R(t) - 5) : SLOT_R(t) < 18 ? 2048 * (SLOT_R(t) - 12) : 0)
    constexpr int HD = 8;
    u32x4 H[28][2], W[28][2];
    auto hload = [&](int t) INL {
#pragma unroll
      for (int sh = 0; sh < 2; ++sh) H[t][sh] = __builtin_amdgcn_raw_buffer_load_b128(rws, hi + sh * 1024, (56 * (t / 14) + t % 14) * 2048, 16);
    };
    auto wload = [&](int t) INL {
#pragma unroll
      for (int x = 0; x < 2; ++x) {
        const unsigned a = slot_b(t) + (w2l ^ (64u * x)) + SLOT_O(t);
        W[t][x] = trfrag(a, a + 1024);
      }
    };
    poll(sws, hpo, tag);
    STAMP(17);
#pragma unroll
    for (int t = 0; t < HD; ++t) hload(t);
    u32x2 fy;
    asm volatile("buffer_load_dwordx2 %0, %1, %2, 0 offen offset:512 sc1" : "=v"(fy) : "v"(hpo), "s"(sws) : "memory");
    wait_prog(pa, 3, 16u << rgw);
    wload(0); wload(1);
    __builtin_amdgcn_s_setprio(2);
    SB();
    STAMP(18);
    sfor<0, 28>([&](auto tc) INL {
      constexpr int t = tc;
      if constexpr (t == 14 - HD) {
        asm volatile("s_waitcnt vmcnt(%1)" : "+v"(fy) : "n"(4 * (14 - HD)) : "memory");
        if (__builtin_amdgcn_ballot_w64(fy[0] != tag[0] || fy[1] != tag[1]) != 0) poll(sws, hpo + 512u, tag);
        STAMP(19);
      }
      if constexpr (t == 10) wait_prog(pa, 5, 16u << rgw);
      if constexpr (t + 2 < 28) wload(t + 2);
      acc[0][0] = mfma_bf(W[t][0], H[t][0], acc[0][0]);
      acc[0][1] = mfma_bf(W[t][0], H[t][1], acc[0][1]);
      SB();
      if constexpr (t + HD < 28) hload(t + HD);
      if constexpr (t == 28 - HD) {
        xres = __builtin_amdgcn_raw_buffer_load_b128(rx, oo, 0, 0);
        b2v = __builtin_amdgcn_raw_buffer_load_b128(rb2, (unsigned)(128 * cg + (tid & 7) * 16), 0, 0);
      }
      SB();
      acc[1][0] = mfma_bf(W[t][1], H[t][0], acc[1][0]);
      acc[1][1] = mfma_bf(W[t][1], H[t][1], acc[1][1]);
      SB();
      if constexpr (t < 9) dma_w2<1>(slot_b(t), SLOT_O(t), vw2, vw2b, sw2, W2O(t + 19));
      if constexpr (t == 17) prog(5);
      if constexpr (t == 13) STAMP(20);
    });
    __builtin_amdgcn_s_setprio(0);
    STAMP(21);
  } else {
    const int rho = rgw & 1, kap = rgw >> 1;
    u32x4 I[15][2];
#pragma unroll
    for (int x = 0; x < 2; ++x) {
      const float d = (float)(64 * cg + 32 * x + l31), dl = d * (1.0f / 448.0f);
      const float tw = 2.0f * cosr(rev448(8.0f * d));
      const float k0 = (float)(112 * kap + 4 * h);
      const float a0 = rev448(k0 * d), a1 = rev448((k0 + 8.0f) * d);
      float f0[8], f1[8];
#pragma unroll
      for (int e = 0; e < 4; ++e) {
        const float b0 = __builtin_fmaf((float)e, dl, a0), b1 = __builtin_fmaf((float)e, dl, a1);
        f0[e] = cosr(b0); f0[4 + e] = sinr(-b0);
        f1[e] = cosr(b1); f1[4 + e] = sinr(-b1);
      }
#pragma unroll
      for (int i = 0; i < 15; ++i) {
#pragma unroll
        for (int e = 0; e < 4; ++e) I[i][x][e] = pk_bf(f0[2 * e], f0[2 * e + 1]);
#pragma unroll
        for (int e = 0; e < 8; ++e) {
          const float n = __builtin_fmaf(tw, f1[e], -f0[e]);
          f0[e] = f1[e];
          f1[e] = n;
        }
        PIN(I[i][x]);
      }
      if (x == 0) {
        WAITVM(1);
        prog(5);
        STAMP(17);
      }
    }
    u32x4 GC[2][2];
#pragma unroll
    for (int sh = 0; sh < 2; ++sh) {
      const float sf = (float)(32 * (32 * sh + l31) + s2) * (1.0f / 2048.0f);
#pragma unroll
      for (int t = 0; t < 2; ++t)
#pragma unroll
        for (int e = 0; e < 4; ++e) {
          const int p = 16 * rho + 8 * t + 4 * (e >> 1) + 2 * h + (e & 1);
          const float a = (float)p * sf;
          float sv = sinr(a);
          if (t == 0 && e == 0) sv = (p == 0) ? ((l31 & 1) ? -1.0f : 1.0f) : sv;
          GC[sh][t][e] = pk_bf(cosr(a), sv);
        }
      PIN(GC[sh][0]); PIN(GC[sh][1]);
    }
    unsigned ai = WS_A + (unsigned)(((s2 * 2 + rho) * 29 + 14 * kap) * 1024 + (l31 >> 3) * 256 + h * 128 + (l31 & 7) * 16);
    PIN(ai);
    STAMP(18);
    poll(sws, WS_AF + (unsigned)(((4 * rho + (lane & 3)) * 4 + 2 * kap + ((lane >> 2) & 1)) * 128), tag);
    STAMP(19);
    u32x4 A[15];
#pragma unroll
    for (int i = 0; i < 15; ++i) A[i] = __builtin_amdgcn_raw_buffer_load_b128(rws, (i == 14 && kap == 0) ? OOB : ai, i * 1024, 16);
    xres = __builtin_amdgcn_raw_buffer_load_b128(rx, oo, 0, 0);
    b2v = __builtin_amdgcn_raw_buffer_load_b128(rb2, (unsigned)(128 * cg + (tid & 7) * 16), 0, 0);
    __builtin_amdgcn_s_setprio(1);
    SB();
    f32x16 T[2] = {};
#pragma unroll
    for (int i = 0; i < 15; ++i) {
      T[0] = mfma_bf(A[i], I[i][0], T[0]);
      T[1] = mfma_bf(A[i], I[i][1], T[1]);
    }
    STAMP(20);
#pragma unroll
    for (int x = 0; x < 2; ++x)
#pragma unroll
      for (int t = 0; t < 2; ++t) {
        u32x4 tf;
#pragma unroll
        for (int e = 0; e < 4; ++e) tf[e] = pk_bf2(pr(T[x], 8 * t + 2 * e));
        acc[x][0] = mfma_bf(tf, GC[0][t], acc[x][0]);
        acc[x][1] = mfma_bf(tf, GC[1][t], acc[x][1]);
      }
    __builtin_amdgcn_s_setprio(0);
    wait_prog(pa, 5, 1u << rgw);
    STAMP(21);
  }
  {
    const unsigned pb = (grp ? bD : bC) + (unsigned)(l31 * 128 + h * 8);
#pragma unroll
    for (int x = 0; x < 2; ++x)
#pragma unroll
      for (int sh = 0; sh < 2; ++sh)
#pragma unroll
        for (int q = 0; q < 4; ++q) {
          const u32x2 v = {pk_bf2(pr(acc[x][sh], 4 * q)), pk_bf2(pr(acc[x][sh], 4 * q + 2))};
          *(LAS u32x2*)(pb + (unsigned)(sh * 4096 + (((4 * x + q) ^ (l31 & 7)) * 16))) = v;
        }
  }
  STAMP(22);
  LBAR();
  {
    const unsigned ra = LB + (unsigned)((tid >> 3) * 128 + (((tid & 7) ^ ((tid >> 3) & 7)) * 16));
    float r[8];
#pragma unroll
    for (int e = 0; e < 4; ++e) {
      r[2 * e] = bf_lo(xres[e]) + bf_lo(b2v[e]);
      r[2 * e + 1] = bf_hi(xres[e]) + bf_hi(b2v[e]);
    }
#pragma unroll
    for (int k = 0; k < 8; ++k) {
      const u32x4 v = lds128(ra + (k < 4 ? L_C0 + k * 14336 : 2048 + (k - 4) * 12288));
#pragma unroll
      for (int e = 0; e < 4; ++e) {
        r[2 * e] += bf_lo(v[e]);
        r[2 * e + 1] += bf_hi(v[e]);
      }
    }
    const u32x4 o = {pk_bf(r[0], r[1]), pk_bf(r[2], r[3]), pk_bf(r[4], r[5]), pk_bf(r[6], r[7])};
    __builtin_amdgcn_raw_buffer_store_b128(o, rout, oo, 0, 16);
  }
  if (blockIdx.x == 0 && tid == 0) __builtin_amdgcn_raw_buffer_store_b64(tag, rws, WS_E, 0, 16);
  STAMP(23);
  STAMP_OUT();
}

extern "C" void kernel_launch(void* const* d_in, const int* in_sizes, int n_in, void* d_out, int out_size,
                              void* d_ws, size_t ws_size, hipStream_t stream) {
  fused_kernel<<<256, 512, 0, stream>>>(d_in[0], d_in[4], d_ws, d_in[6], d_in[1], d_in[2], d_in[5], d_in[7], d_out);
}
```

```cpp
#include <hip/hip_runtime.h>
#include <stdint.h>
#include <type_traits>
#pragma clang diagnostic ignored "-Wint-to-pointer-cast"
#pragma clang diagnostic ignored "-Winline-asm"

#define DEV __device__ __forceinline__
#define INL __attribute__((always_inline))
#if defined(__HIP_DEVICE_COMPILE__)
#define NOPK __attribute__((target("no-packed-fp32-ops")))
#else
#define NOPK
#endif
#define LAS __attribute__((address_space(3)))
typedef __attribute__((ext_vector_type(4))) short s16x4;
typedef __attribute__((ext_vector_type(8))) short s16x8;
typedef __attribute__((ext_vector_type(8))) __bf16 bf16x8;
typedef __attribute__((ext_vector_type(2))) __bf16 bf16x2;
typedef __attribute__((ext_vector_type(8))) _Float16 f16x8;
typedef __attribute__((ext_vector_type(2))) _Float16 f16x2;
typedef __attribute__((ext_vector_type(2))) float f32x2;
typedef __attribute__((ext_vector_type(4))) float f32x4;
typedef __attribute__((ext_vector_type(16))) float f32x16;
typedef __attribute__((ext_vector_type(2))) unsigned u32x2;
typedef __attribute__((ext_vector_type(4))) unsigned u32x4;
typedef __attribute__((ext_vector_type(16))) unsigned u32x16;
typedef __amdgpu_buffer_rsrc_t rsrc_t;

constexpr unsigned WS_E = 0;
constexpr unsigned WS_ZF = 4096;
constexpr unsigned WS_HF = WS_ZF + 224 * 512;
constexpr unsigned WS_AF = WS_HF + 224 * 1024;
constexpr unsigned WS_Z = 393216;
constexpr unsigned WS_A = WS_Z + 29 * 16 * 4096;
constexpr unsigned WS_H = WS_A + 32 * 2 * 29 * 1024;
constexpr unsigned WS_END = WS_H + 32 * 112 * 2048;
constexpr unsigned OOB = 0x80000000u;
constexpr int SPIN_CAP = 1 << 16;
#define POLL_SLEEP 8
#define XUP 3
#define W_AUX 0
#define CODE_BYTES 29696

constexpr unsigned L_PROG = 0;
constexpr unsigned L_RS = 512;
constexpr unsigned L_XA = 1024;
constexpr unsigned L_B1 = 1536, L_B2 = 1792;
constexpr unsigned L_C0 = 57344;
constexpr unsigned L_E = 114688;
constexpr unsigned L_GATH = L_E + 32768;
constexpr unsigned L_CS = L_E + 4096;
DEV constexpr unsigned xblk(int kc) { return kc < 4 ? 2048u + 16384u * kc : L_E + 16384u * (kc - 4); }

#define WAITBAR(n) asm volatile("s_waitcnt vmcnt(" #n ") lgkmcnt(0)\n\ts_barrier" ::: "memory")
#define LBAR() asm volatile("s_waitcnt lgkmcnt(0)\n\ts_barrier" ::: "memory")
#define PIN(x) asm volatile("" : "+v"(x))
#define SB() __builtin_amdgcn_sched_barrier(0)
#define STAMP(i)
#define STAMP_OUT()
#define PRESTAMP()
#define STAMP2(i, k)
#define WAITVM(n) asm volatile("s_waitcnt vmcnt(" #n ")" ::: "memory")

DEV u32x4 mk_srd(const void* p, unsigned bytes) {
  uint64_t a = (uint64_t)p;
  u32x4 r = {(unsigned)a, (unsigned)(a >> 32) & 0xffffu, bytes, 0x00020000u};
  return r;
}
DEV void dma16(unsigned lds, unsigned voff, u32x4 srd, unsigned soff) {
  asm volatile("s_mov_b32 m0, %0\n\ts_nop 0\n\tbuffer_load_dwordx4 %1, %2, %3 offen lds"
               :: "s"(lds), "v"(voff), "s"(srd), "s"(soff) : "memory");
}
DEV u32x4 lds128(unsigned a) { return *(LAS u32x4*)a; }
DEV void lds128w(unsigned a, u32x4 v) { *(LAS u32x4*)a = v; }
DEV u32x4 trfrag(unsigned a0, unsigned a1) {
  s16x4 lo = __builtin_amdgcn_ds_read_tr16_b64_v4i16((LAS s16x4*)a0);
  s16x4 hi = __builtin_amdgcn_ds_read_tr16_b64_v4i16((LAS s16x4*)a1);
  return __builtin_bit_cast(u32x4, __builtin_shufflevector(lo, hi, 0, 1, 2, 3, 4, 5, 6, 7));
}
DEV f32x16 mfma_bf(u32x4 a, u32x4 b, f32x16 c) {
  return __builtin_amdgcn_mfma_f32_32x32x16_bf16(__builtin_bit_cast(bf16x8, a), __builtin_bit_cast(bf16x8, b), c, 0, 0, 0);
}
DEV f32x16 mfma_h(u32x4 a, u32x4 b, f32x16 c) {
  return __builtin_amdgcn_mfma_f32_32x32x16_f16(__builtin_bit_cast(f16x8, a), __builtin_bit_cast(f16x8, b), c, 0, 0, 0);
}
DEV unsigned pk_bf(float lo, float hi) {
  f32x2 p = {lo, hi};
  return __builtin_bit_cast(unsigned, __builtin_convertvector(p, bf16x2));
}
DEV unsigned pk_h(float lo, float hi) {
  f32x2 p = {lo, hi};
  return __builtin_bit_cast(unsigned, __builtin_convertvector(p, f16x2));
}
DEV unsigned pk_bf2(f32x2 p) { return __builtin_bit_cast(unsigned, __builtin_convertvector(p, bf16x2)); }
DEV unsigned pk_h2(f32x2 p) { return __builtin_bit_cast(unsigned, __builtin_convertvector(p, f16x2)); }
DEV f32x2 pr(const f32x16& v, int r) { return f32x2{v[r], v[r + 1]}; }
DEV f32x2 fma2(f32x2 a, f32x2 b, f32x2 c) { return __builtin_elementwise_fma(a, b, c); }
DEV f32x2 bc(float a) { return f32x2{a, a}; }
DEV float bf_lo(unsigned v) { return __builtin_bit_cast(float, v << 16); }
DEV float bf_hi(unsigned v) { return __builtin_bit_cast(float, v & 0xffff0000u); }
DEV float bf16r(float x) { return bf_lo(pk_bf(x, 0.f)); }
DEV float dot2(unsigned a, unsigned b, float c) {
  return __builtin_amdgcn_fdot2_f32_bf16(__builtin_bit_cast(bf16x2, a), __builtin_bit_cast(bf16x2, b), c, false);
}
DEV float cosr(float rev) { return __builtin_amdgcn_cosf(rev); }
DEV float sinr(float rev) { return __builtin_amdgcn_sinf(rev); }
DEV float rev448(float n) {
  float q = __builtin_floorf(n * (1.0f / 448.0f));
  return __builtin_fmaf(q, -448.0f, n) * (1.0f / 448.0f);
}
DEV void poll(u32x4 srd, unsigned voff, u32x2 tag) {
#pragma unroll 1
  for (int n = 0; n < SPIN_CAP; ++n) {
    u32x2 v;
    asm volatile("buffer_load_dwordx2 %0, %1, %2, 0 offen sc1\n\ts_waitcnt vmcnt(0)"
                 : "=&v"(v) : "v"(voff), "s"(srd) : "memory");
    if (__builtin_amdgcn_ballot_w64(v[0] != tag[0] || v[1] != tag[1]) == 0) break;
    __builtin_amdgcn_s_sleep(POLL_SLEEP);
  }
}
DEV u32x2 mk_tag(u32x2 ev) {
  uint64_t e = ((uint64_t)__builtin_amdgcn_readfirstlane(ev[1]) << 32) | (unsigned)__builtin_amdgcn_readfirstlane(ev[0]);
  e = e * 6364136223846793005ull + 1442695040888963407ull;
  return u32x2{(unsigned)e, (unsigned)(e >> 32)};
}
template <int A, int B, class Fn> DEV void sfor(Fn&& f) {
  if constexpr (A < B) { f(std::integral_constant<int, A>{}); sfor<A + 1, B>(f); }
}
DEV int shx(int v, int m) { return __builtin_amdgcn_ds_bpermute((int)((__builtin_amdgcn_mbcnt_hi(~0u, __builtin_amdgcn_mbcnt_lo(~0u, 0u)) ^ m) << 2), v); }
DEV float xor_add(float v, int m) { return v + __builtin_bit_cast(float, shx(__builtin_bit_cast(int, v), m)); }
DEV int imin(int a, int b) { return a < b ? a : b; }

DEV void dma_x(unsigned lds, unsigned lit, unsigned v0, unsigned v1, unsigned v2, unsigned v3, u32x4 srd, unsigned& col) {
  unsigned t;
  asm volatile("s_add_u32 m0, %2, %3\n\ts_add_u32 %1, %0, 128\n\t"
               "buffer_load_dwordx4 %4, %8, %0 offen lds\n\t"
               "buffer_load_dwordx4 %5, %8, %0 offen offset:1024 lds\n\t"
               "buffer_load_dwordx4 %6, %8, %0 offen offset:2048 lds\n\t"
               "buffer_load_dwordx4 %7, %8, %0 offen offset:3072 lds\n\t"
               "s_sub_u32 %0, %0, 768\n\ts_min_u32 %0, %0, %1"
               : "+s"(col), "=&s"(t) : "s"(lds), "n"(lit), "v"(v0), "v"(v1), "v"(v2), "v"(v3), "s"(srd) : "memory", "scc");
}
DEV void dma_w1(unsigned lds, unsigned lit, unsigned v, u32x4 srd, unsigned& row) {
  unsigned t;
  asm volatile("s_add_u32 m0, %2, %3\n\ts_add_u32 %1, %0, 114688\n\t"
               "buffer_load_dwordx4 %4, %5, %0 offen lds\n\t"
               "s_sub_u32 %0, %0, 1490944\n\ts_min_u32 %0, %0, %1"
               : "+s"(row), "=&s"(t) : "s"(lds), "n"(lit), "v"(v), "s"(srd) : "memory", "scc");
}
template <int N> DEV void dma_w2(unsigned lds, unsigned lit, unsigned v0, unsigned v1, u32x4 srd, unsigned so) {
  asm volatile("s_add_u32 m0, %1, %2\n\ts_nop 0\n\t"
               "buffer_load_dwordx4 %3, %5, %0 offen lds\n\t"
               "buffer_load_dwordx4 %4, %5, %0 offen offset:1024 lds\n\t"
               ".if %6 > 1\n\ts_add_u32 %0, %0, 12288\n\t"
               "buffer_load_dwordx4 %3, %5, %0 offen offset:2048 lds\n\t"
               "buffer_load_dwordx4 %4, %5, %0 offen offset:3072 lds\n\t.endif"
               : "+s"(so) : "s"(lds), "n"(lit), "v"(v0), "v"(v1), "s"(srd), "n"(N) : "memory", "scc");
}
DEV void touch(unsigned lds, unsigned voff, u32x4 srd) {
  asm volatile("s_mov_b32 m0, %0\n\ts_nop 0\n\tbuffer_load_dword %1, %2, 0 offen lds" :: "s"(lds), "v"(voff), "s"(srd) : "memory");
}
DEV uint64_t landed() {
  uint64_t e;
  asm volatile("s_waitcnt lgkmcnt(0)\n\ts_mov_b64 %0, s[100:101]" : "=s"(e));
  return e;
}
DEV u32x2 get_tag() {
  uint64_t e = landed();
  e = e * 6364136223846793005ull + 1442695040888963407ull;
  asm volatile("" : "+s"(e));
  return u32x2{(unsigned)e, (unsigned)(e >> 32)};
}
DEV void* argp(const u32x16& k, int i) { return (void*)(((uint64_t)k[2 * i + 1] << 32) | k[2 * i]); }
template <int NEED> DEV void wait_prog_(unsigned a, unsigned mask) {
  unsigned v, n, t;
  asm volatile("s_movk_i32 %1, 0x7fff\n"
               "1:\tds_read_b32 %0, %3\n\ts_sub_u32 %1, %1, 1\n\ts_waitcnt lgkmcnt(0)\n\ts_cbranch_scc1 2f\n\t"
               "v_cmp_gt_u32 vcc, %5, %0\n\ts_and_b32 %2, vcc_lo, %4\n\ts_cbranch_scc1 1b\n2:"
               : "=&v"(v), "=&s"(n), "=&s"(t) : "v"(a), "s"(mask), "n"(NEED) : "memory", "scc", "vcc");
}
#define wait_prog(a, need, mask) wait_prog_<need>(a, mask)
template <int NEED> DEV void check_prog(unsigned v, unsigned a, unsigned mask) {
  unsigned n, t;
  asm volatile("s_movk_i32 %1, 0x7fff\n\ts_branch 3f\n"
               "1:\tds_read_b32 %0, %3\n\ts_sub_u32 %1, %1, 1\n\ts_waitcnt lgkmcnt(0)\n\ts_cbranch_scc1 2f\n"
               "3:\tv_cmp_gt_u32 vcc, %5, %0\n\ts_and_b32 %2, vcc_lo, %4\n\ts_cbranch_scc1 1b\n2:"
               : "+v"(v), "=&s"(n), "=&s"(t) : "v"(a), "s"(mask), "n"(NEED) : "memory", "scc", "vcc");
}
template <int NEED> DEV void wait_one(unsigned a, unsigned& have) {
  unsigned v, n;
  asm volatile("s_cmp_ge_u32 %2, %4\n\ts_cbranch_scc1 2f\n\ts_movk_i32 %1, 0x7fff\n"
               "1:\tds_read_b32 %0, %3\n\ts_sub_u32 %1, %1, 1\n\ts_waitcnt lgkmcnt(0)\n\ts_cbranch_scc1 2f\n\t"
               "v_readfirstlane_b32 %2, %0\n\ts_cmp_lt_u32 %2, %4\n\ts_cbranch_scc1 1b\n2:"
               : "=&v"(v), "=&s"(n), "+s"(have) : "v"(a), "n"(NEED) : "memory", "scc");
}
DEV void flag_store(rsrc_t r, unsigned off, int lane, u32x2 tag) {
  __builtin_amdgcn_raw_buffer_store_b128(u32x4{tag[0], tag[1], tag[0], tag[1]}, r, lane < 8 ? off + (unsigned)lane * 16u : OOB, 0, 16);
}
DEV f32x4 mfma4(unsigned a0, unsigned a1, unsigned b0, unsigned b1, f32x4 c) {
  return __builtin_amdgcn_mfma_f32_4x4x4bf16_1k(__builtin_bit_cast(s16x4, u32x2{a0, a1}), __builtin_bit_cast(s16x4, u32x2{b0, b1}), c, 0, 0, 0);
}
DEV f32x4 mfma16(u32x4 a, u32x4 b, f32x4 c) {
  return __builtin_amdgcn_mfma_f32_16x16x32_bf16(__builtin_bit_cast(bf16x8, a), __builtin_bit_cast(bf16x8, b), c, 0, 0, 0);
}
DEV float half_sum(float v) {
  const unsigned u = __builtin_bit_cast(unsigned, v);
  const auto r = __builtin_amdgcn_permlane32_swap(u, u, false, false);
  return __builtin_bit_cast(float, (unsigned)r[0]) + __builtin_bit_cast(float, (unsigned)r[1]);
}
DEV f16x2 h2(float a, float b) { return f16x2{(_Float16)a, (_Float16)b}; }
DEV f16x2 hfma(f16x2 a, f16x2 b, f16x2 c) { return __builtin_elementwise_fma(a, b, c); }

__global__ void NOPK __launch_bounds__(512, 2)
fused_kernel(const void* a_x, const void* a_w1, void* a_ws, const void* a_w2, const void* a_b1, const void* a_b2,
             void* a_out, const void* a_wr, const void* a_wi) {
  __shared__ __attribute__((aligned(1024))) char smem[163840];
  const unsigned LB = (unsigned)(uintptr_t)(LAS char*)smem;
  const int tid = threadIdx.x;
  const int lane = tid & 63;
  const int w = __builtin_amdgcn_readfirstlane(tid >> 6);
  const int l31 = lane & 31, h = lane >> 5;
  const int xcd = blockIdx.x & 7, loc = blockIdx.x >> 3;
  u32x16 ka;
  const auto kseg = __builtin_amdgcn_kernarg_segment_ptr();
  asm volatile("s_cmp_lg_u32 %1, 0\n\ts_cbranch_scc1 1f\n\ts_load_dwordx2 s[100:101], %0, 0\n\ts_load_dwordx2 s[98:99], %0, 64\n1:"
               :: "s"(kseg), "s"(loc < 28 ? w >> 2 : 0) : "scc", "s98", "s99", "s100", "s101");
#define ARGS_LOAD() asm volatile("s_load_dwordx16 %0, %1, 0\n\ts_waitcnt lgkmcnt(0)" : "=&s"(ka) : "s"(kseg) : "memory")
#define TAG_LOAD() asm volatile("s_load_dwordx2 s[100:101], %0, 0" :: "s"(argp(ka, 2)) : "s100", "s101")
  if (w == 4)
    touch(LB + 256, (unsigned)(loc + 32 * lane) * 128u, mk_srd((const void*)__builtin_amdgcn_s_getpc(), CODE_BYTES));

  if (__builtin_expect(loc >= 28, 0)) {
    const int pq = xcd, qt = loc - 28;
    const int ch = tid & 7, rw = tid >> 3;
    unsigned wo[4];
#pragma unroll
    for (int i = 0; i < 4; ++i) {
      const int idx = rw + 64 * (i & 1), p = 4 * pq + (idx & 3), k2 = idx >> 2;
      const int k = (i >> 1) ? (p ? ((2048 - p + 64 * k2) & 2047) : (32 + 64 * k2)) : (p + 64 * k2);
      wo[i] = (ch < 7 || qt == 3) ? (unsigned)(k * 225 + 56 * qt + (ch < 7 ? 8 * ch : 49)) * 2u : OOB;
      PIN(wo[i]);
    }
    ARGS_LOAD();
    uint64_t pwi;
    asm volatile("s_mov_b64 %0, s[98:99]" : "=s"(pwi));
    void* pws = argp(ka, 2);
    const u32x4 sws = mk_srd(pws, WS_END);
    const rsrc_t rws = __builtin_amdgcn_make_buffer_rsrc(pws, 0, WS_END, 0x00020000);
    const rsrc_t rwr = __builtin_amdgcn_make_buffer_rsrc(argp(ka, 7), 0, 2048 * 225 * 2, 0x00020000);
    const rsrc_t rwi = __builtin_amdgcn_make_buffer_rsrc((void*)pwi, 0, 2048 * 225 * 2, 0x00020000);
    STAMP(0);
    {
      const u32x4 sw1 = mk_srd(argp(ka, 1), 448 * 1792 * 2);
      const unsigned o = (lane & 7) < 7 && lane < 56 ? (unsigned)((112 * qt + 14 * w + (lane >> 3)) * 3584 + 896 * (xcd & 1) + 128 * (lane & 7)) : OOB;
      touch(LB + 98304, o, sw1);
      touch(LB + 98304, o + 7 * 3584, sw1);
    }
    u32x4 wv[8];
#pragma unroll
    for (int i = 0; i < 4; ++i) {
      wv[i] = __builtin_amdgcn_raw_buffer_load_b128(rwr, wo[i], 0, W_AUX);
      wv[4 + i] = __builtin_amdgcn_raw_buffer_load_b128(rwi, wo[i], 0, W_AUX);
    }
    TAG_LOAD();
    SB();
    STAMP(1);
    const bool active = (w < 7) || (qt == 3);
    const int g = 7 * qt + w;
    const int kdl = lane & 7, pl = (lane >> 3) & 3;
    const int p = 4 * pq + pl, kd = 8 * g + kdl;
    u32x4 FR[4], FI[4], IR[4], II[4];
#pragma unroll
    for (int t = 0; t < 4; ++t) {
      float c[4], sn[4];
#pragma unroll
      for (int e = 0; e < 4; ++e) {
        const float a = (float)(l31 * (8 * t + 4 * h + e)) * (1.0f / 32.0f);
        c[e] = cosr(a);
        sn[e] = sinr(a);
        FR[t][e] = pk_h(c[e], sn[e]);
        FI[t][e] = pk_h(-sn[e], c[e]);
      }
      IR[t] = u32x4{pk_bf(c[0], c[1]), pk_bf(c[2], c[3]), pk_bf(-sn[0], -sn[1]), pk_bf(-sn[2], -sn[3])};
      II[t] = u32x4{pk_bf(sn[0], sn[1]), pk_bf(sn[2], sn[3]), pk_bf(c[0], c[1]), pk_bf(c[2], c[3])};
      PIN(FR[t]); PIN(FI[t]); PIN(IR[t]); PIN(II[t]);
    }
#pragma unroll
    for (int i = 0; i < 8; ++i) lds128w(LB + (unsigned)(((i >> 1) * 128 + rw + 64 * (i & 1)) * 144 + ch * 16), wv[i]);
    const u32x2 tag = get_tag();
    LBAR();
    if (active) {
      f32x2 wpr[8], wpi[8], wmr[8], wmi[8];
      const float wsc = ((kd == 0 || kd == 224) ? 1.0f : 2.0f) / (2048.0f * 448.0f);
      const unsigned wa = LB + (unsigned)((16 * h + pl) * 144 + (w < 7 ? 8 * w + kdl : 63) * 2);
#pragma unroll
      for (int r = 0; r < 16; ++r) {
        const unsigned o = (unsigned)(((r & 3) + 8 * (r >> 2)) * 4 * 144);
        wpr[r >> 1][r & 1] = bf_lo(*(LAS unsigned short*)(wa + o));
        wmr[r >> 1][r & 1] = bf_lo(*(LAS unsigned short*)(wa + o + 128 * 144));
        wpi[r >> 1][r & 1] = bf_lo(*(LAS unsigned short*)(wa + o + 256 * 144));
        wmi[r >> 1][r & 1] = bf_lo(*(LAS unsigned short*)(wa + o + 384 * 144));
      }
      float ff = pq ? 1.0f : 0.0f;
      float cph = pq ? 1.0f : cosr((float)l31 * (1.0f / 64.0f));
      float sph = pq ? 0.0f : sinr((float)l31 * (1.0f / 64.0f));
      PIN(ff); PIN(cph); PIN(sph);
#pragma unroll
      for (int r = 0; r < 8; ++r) {
        wpr[r] *= bc(wsc); wpi[r] *= bc(wsc); wmr[r] *= bc(wsc); wmi[r] *= bc(wsc);
        PIN(wpr[r]); PIN(wpi[r]); PIN(wmr[r]); PIN(wmi[r]);
      }
      unsigned zo = (kd > 224) ? OOB : WS_Z + (unsigned)((g * 16 + 2 * h) * 4096 + p * 128 + kdl * 16);
      unsigned ao = WS_A + (unsigned)((((lane >> 4) * 2 + (pq >> 2)) * 29 + g) * 1024 + (pq & 3) * 256 + (lane & 15) * 16);
      unsigned tw = LB + 90112u + (unsigned)(w * 8192 + l31 * 256);
      unsigned tr = LB + 90112u + (unsigned)(w * 8192 + (lane >> 4) * 256);
      PIN(zo); PIN(ao); PIN(tw); PIN(tr);
      STAMP(2);
      const int jz = (g == 28) ? 0 : (g >> 1);
      poll(sws, WS_ZF + (unsigned)(((lane & 15) * 14 + jz) * 512 + (pq >> 1) * 128), tag);
      STAMP(3);

      u32x4 z[8];
#pragma unroll
      for (int i = 0; i < 8; ++i)
        z[i] = __builtin_amdgcn_raw_buffer_load_b128(rws, zo, (4 * (i >> 1) + (i & 1)) * 4096, 16);
      SB();
      f32x16 xpr = {}, xpi = {}, xmr = {}, xmi = {};
#pragma unroll
      for (int t = 0; t < 4; ++t) {
        const u32x4 zp = {z[2 * t][0], z[2 * t][1], z[2 * t + 1][0], z[2 * t + 1][1]};
        xpr = mfma_h(FR[t], zp, xpr);
        xpi = mfma_h(FI[t], zp, xpi);
      }
#pragma unroll
      for (int t = 0; t < 4; ++t) {
        const u32x4 zm = {z[2 * t][2], z[2 * t][3], z[2 * t + 1][2], z[2 * t + 1][3]};
        xmr = mfma_h(FR[t], zm, xmr);
        xmi = mfma_h(FI[t], zm, xmi);
      }
      STAMP(4);
      f32x16 ypr = {}, ypi = {}, ymr = {}, ymi = {};
#pragma unroll
      for (int t = 0; t < 4; ++t) {
        u32x4 pp;
#pragma unroll
        for (int e = 0; e < 2; ++e) {
          const int r = 4 * t + 2 * e, q = 2 * t + e;
          pp[e] = pk_bf2(fma2(pr(xpr, r), wpr[q], -(pr(xpi, r) * wpi[q])));
          pp[2 + e] = pk_bf2(fma2(pr(xpr, r), wpi[q], pr(xpi, r) * wpr[q]));
        }
        ypr = mfma_bf(pp, IR[t], ypr);
        ypi = mfma_bf(pp, II[t], ypi);
      }
#pragma unroll
      for (int t = 0; t < 4; ++t) {
        u32x4 pm;
#pragma unroll
        for (int e = 0; e < 2; ++e) {
          const int r = 4 * t + 2 * e, q = 2 * t + e;
          pm[e] = pk_bf2(fma2(pr(xmr, r), wmr[q], -(pr(xmi, r) * wmi[q])));
          pm[2 + e] = pk_bf2(fma2(pr(xmr, r), wmi[q], pr(xmi, r) * wmr[q]));
        }
        ymr = mfma_bf(pm, IR[t], ymr);
        ymi = mfma_bf(pm, II[t], ymi);
      }
      STAMP(5);
#pragma unroll
      for (int q = 0; q < 4; ++q) {
        u32x4 uc, us;
#pragma unroll
        for (int e = 0; e < 2; ++e) {
          const int r = 4 * q + 2 * e;
          f32x2 acr, aci, asr, asi;
          if (q == 0) {
            acr = fma2(bc(ff), pr(ymr, r), pr(ypr, r));
            aci = fma2(bc(ff), pr(ymi, r), pr(ypi, r));
            asr = fma2(bc(cph), pr(ymi, r), fma2(bc(sph), pr(ymr, r), -(bc(ff) * pr(ypi, r))));
            asi = fma2(bc(sph), pr(ymi, r), fma2(bc(ff), pr(ypr, r), -(bc(cph) * pr(ymr, r))));
          } else {
            acr = pr(ypr, r) + pr(ymr, r);
            aci = pr(ypi, r) + pr(ymi, r);
            asr = pr(ymi, r) - pr(ypi, r);
            asi = pr(ypr, r) - pr(ymr, r);
          }
          uc[e] = pk_bf2(acr); uc[2 + e] = pk_bf2(aci);
          us[e] = pk_bf2(asr); us[2 + e] = pk_bf2(asi);
        }
        lds128w(tw + (unsigned)(((8 * h + 2 * q) ^ (l31 & 7)) * 16), uc);
        lds128w(tw + (unsigned)(((8 * h + 2 * q + 1) ^ (l31 & 7)) * 16), us);
      }
#pragma unroll
      for (int i = 0; i < 8; ++i) {
        const u32x4 v = lds128(tr + (unsigned)(i * 1024 + (((lane & 15) ^ ((4 * i + (lane >> 4)) & 7)) * 16)));
        __builtin_amdgcn_raw_buffer_store_b128(v, rws, ao + i * 8 * 29 * 1024, 0, 16);
      }
      STAMP(6);
    }
    WAITBAR(0);
    if (w == 0) flag_store(rws, WS_AF + (unsigned)((pq * 4 + qt) * 128), lane, tag);
    STAMP(7);
    STAMP_OUT();
    return;
  }

  auto prog = [&](unsigned v) INL {
    asm volatile("" ::: "memory");
    *(volatile LAS unsigned*)(LB + L_PROG + w * 4) = v;
    asm volatile("" ::: "memory");
  };
  if (w >= 4) {
    *(volatile LAS unsigned*)(LB + L_PROG + (lane & 7) * 4) = 0;
    asm volatile("s_waitcnt lgkmcnt(0)" ::: "memory");
  }
  asm volatile("s_barrier" ::: "memory");
  const int q4 = (loc * 37) >> 8, j7 = loc - 7 * q4;
  const int rg = 4 * (xcd >> 1) + q4, j = 7 * (xcd & 1) + j7;
  const int rgw = w & 3, grp = w >> 2;
  const unsigned rot = __builtin_elementwise_min((unsigned)(j7 + 2 * q4), (unsigned)(j7 + 2 * q4 - 7));
  const int s2 = 2 * rg + (xcd & 1), cg = j7;
  u32x4 sw1, sw2, sws;
  rsrc_t rws;
  auto args = [&]() INL {
    sw1 = mk_srd(argp(ka, 1), 448 * 1792 * 2);
    sw2 = mk_srd(argp(ka, 3), 448 * 1792 * 2);
    sws = mk_srd(argp(ka, 2), WS_END);
    rws = __builtin_amdgcn_make_buffer_rsrc(argp(ka, 2), 0, WS_END, 0x00020000);
  };
  const unsigned swz16 = (unsigned)(((lane & 7) ^ ((lane >> 2) & 4)) * 16);
  const unsigned vw1 = (unsigned)((lane >> 3) * 3584 + 128 * j + 1792 * grp) + swz16;
  const unsigned vw2 = (unsigned)(((lane >> 3) + 224 * rgw) * 896 + 128 * cg) + swz16, vw2b = vw2 + 6144u;
  const unsigned bE = LB + L_E + rgw * 4096, bD = LB + 16384 + rgw * 10240, bF = LB + rgw * 4096;
  const unsigned pa = LB + L_PROG + (lane & 7) * 4;
  const unsigned wr0 = 12u + (unsigned)rgw + 8u * rot;
  unsigned wrow = __builtin_elementwise_min(wr0, wr0 - 56u) * 28672u;
  const unsigned lw = LB + (grp ? 0u : L_C0) + rgw * 1024;
  auto wdma = [&](auto rc) INL {
    constexpr int r = rc;
    if constexpr (r == 11) dma_w1((grp && rgw < 2) ? lw + L_E : lw, 0, vw1, sw1, wrow);
    else dma_w1(lw, ((12 + 4 * r) % 56) * 1024, vw1, sw1, wrow);
  };

  const int tq = (lane >> 2) & 3, tp = lane & 3, tg = (lane >> 4) & 1;
  unsigned w1a[6], oo, o0, o1;
  auto g1setup = [&]() INL {
    const unsigned w1l = (unsigned)((8 * h + tq) * 128 + 64 * (tq >> 1) + 32 * tg + 8 * tp);
    w1a[0] = LB + (grp ? 0u : L_C0) + w1l;
    w1a[2] = LB + (grp ? L_E : L_C0) + w1l;
    w1a[4] = w1a[0] + (unsigned)rgw * 14336u;
#pragma unroll
    for (int i = 0; i < 6; i += 2) { w1a[i + 1] = w1a[i] ^ 64u; PIN(w1a[i]); PIN(w1a[i + 1]); }
    const int sel = (lane & 15) - 4 * tg;
    o0 = sel == 0 ? 0x3f803f80u : 0u, o1 = sel == 8 ? 0x3f803f80u : 0u;
    oo = (unsigned)((32 * (tid >> 3) + s2) * 896 + 128 * cg + (tid & 7) * 16);
    PIN(o0); PIN(o1); PIN(oo);
  };
  u32x4 X[28];
  unsigned xa4[2][4];
  auto xaddr = [&]() INL {
#pragma unroll
    for (int u = 0; u < 8; ++u) {
      xa4[u >> 2][u & 3] = LB + (unsigned)((32 * rgw + l31) * 128 + (((h ^ ((l31 >> 1) & 7)) ^ (2 * (u & 3))) * 16)) + xblk(u & 4);
      PIN(xa4[u >> 2][u & 3]);
    }
  };
  auto xread = [&](int u) INL { X[u] = lds128(xa4[u >> 4][u & 3] + 16384u * ((u >> 2) & 3)); };
  u32x2 tag;
  u32x4 xres;
  unsigned ms;
  float rstd;

  if (__builtin_expect(grp == 0, 1)) {
    unsigned vx0 = (unsigned)((32 * (32 * (rgw & 1) + (lane >> 3)) + 2 * rg + (rgw >> 1)) * 896 + (((lane & 7) ^ (lane >> 4)) * 16));
    unsigned vx1 = (vx0 ^ 64u) + 228352u, vx2 = vx0 + 2 * 228352u, vx3 = vx1 + 2 * 228352u;
    unsigned xcol = rot * 128u, lx = LB + rgw * 4096;
    PIN(vx0); PIN(vx1); PIN(vx2); PIN(vx3);
    asm volatile("s_setprio 3" : "+s"(xcol), "+s"(lx) :: "memory");
    const u32x4 sx = mk_srd((const void*)landed(), 2048 * 448 * 2);
    auto xdma = [&](auto kc) INL { dma_x(lx, xblk(kc), vx0, vx1, vx2, vx3, sx, xcol); };
    PRESTAMP();
    sfor<0, XUP>(xdma);
    ARGS_LOAD();
    TAG_LOAD();
    SB();
    touch(LB + L_B1, (unsigned)(128 * j + 1792 * h + 4 * l31), mk_srd(argp(ka, 4), 1792 * 2));
    touch(LB + L_B2, (unsigned)(128 * cg + 4 * lane), mk_srd(argp(ka, 5), 448 * 2));
    args();
    xaddr();
    unsigned vt2 = lane < 14 ? (unsigned)((56 * (4 * xcd + q4) + 14 * rgw + lane) * 896 + 128 * cg) : OOB;
    PIN(vt2);
    const unsigned tr0 = 96u + 64u * rot + (unsigned)(32 * rgw + l31);
    unsigned vt = h ? OOB : __builtin_elementwise_min(tr0, tr0 - 448u) * 3584u + (unsigned)(128 * j + 1792);
    PIN(vt);
    g1setup();
    unsigned sta = LB + (unsigned)(32 * rgw + l31) * 4u;
    PIN(sta);
    f32x4 sa = {}, sb = {}, qa = {}, qb = {};
    sfor<0, 7>([&](auto ic) INL {
      constexpr int i = ic;
      asm volatile("s_waitcnt vmcnt(%0)" :: "n"(i < 3 ? 10 : i < 6 ? 8 : 9) : "memory");
      prog(i + 1);
      if constexpr (i == 6) STAMP(4);
#pragma unroll
      for (int u = 4 * i; u < 4 * i + 4; ++u) xread(u);
      SB();
      if constexpr (i < 7 - XUP) xdma(std::integral_constant<int, i + XUP>{});
      if constexpr (i == 4) sfor<0, 4>(wdma);
      if constexpr (i == 5) { touch(LB + 256, vt, sw1); touch(LB + 256, vt2, sw2); sfor<4, 7>(wdma); }
      SB();
#pragma unroll
      for (int u = 4 * i; u < 4 * i + 4; ++u) {
        sa = mfma4(0x3f803f80u, 0x3f803f80u, X[u][0], X[u][1], sa);
        qa = mfma4(X[u][0], X[u][1], X[u][0], X[u][1], qa);
        sb = mfma4(0x3f803f80u, 0x3f803f80u, X[u][2], X[u][3], sb);
        qb = mfma4(X[u][2], X[u][3], X[u][2], X[u][3], qb);
      }
      SB();
    });
    qa += qb;
    const float q01 = (lane & 1) ? qa[1] : qa[0], q23 = (lane & 1) ? qa[3] : qa[2];
    const float mean = half_sum(sa[0] + sb[0]) * (1.0f / 448.0f);
    const float var = __builtin_fmaxf(__builtin_fmaf(-mean, mean, half_sum((lane & 2) ? q23 : q01) * (1.0f / 448.0f)), 0.0f);
    ms = pk_bf(-mean, __builtin_amdgcn_sqrtf(bf16r(bf16r(var) + 1e-5f)));
    rstd = __builtin_amdgcn_rcpf(bf_hi(ms));
    *(LAS unsigned*)(sta + L_XA) = ms;
    *(LAS float*)(sta + L_RS) = rstd;
    prog(8);
    __builtin_amdgcn_s_setprio(0);
    STAMP(5);
    sfor<7, 11>(wdma);
    wait_prog(pa, 5, 0xff);
    sfor<11, 14>(wdma);
    tag = get_tag();
    WAITVM(7);
    prog(9);
    STAMP(6);
    wait_prog(pa, 9, 0x0f);
    __builtin_amdgcn_s_setprio(2);
  } else {
    u32x4 F[28];
    {
      const bool sp = (j == 0) && (l31 == 16);
      const float kdf = sp ? 224.0f : (float)(16 * j + (l31 & 15));
      const float ph = ((l31 >> 4) && !sp) ? 0.25f : 0.0f;
      const float tw = 2.0f * cosr(rev448(16.0f * kdf));
      const float d0 = (float)(8 * h + 64 * (int)rot), dl = kdf * (1.0f / 448.0f);
      const float a0 = rev448(d0 * kdf) + ph, a1 = rev448((d0 + 16.0f) * kdf) + ph;
      float f0[8], f1[8];
#pragma unroll
      for (int e = 0; e < 8; ++e) {
        f0[e] = cosr(__builtin_fmaf((float)e, dl, a0));
        f1[e] = cosr(__builtin_fmaf((float)e, dl, a1));
      }
#pragma unroll
      for (int i = 0; i < 28; ++i) {
#pragma unroll
        for (int e = 0; e < 4; ++e) F[i][e] = pk_bf(f0[2 * e], f0[2 * e + 1]);
#pragma unroll
        for (int e = 0; e < 8; ++e) {
          const float n = __builtin_fmaf(tw, f1[e], -f0[e]);
          f0[e] = f1[e];
          f1[e] = n;
        }
        PIN(F[i]);
      }
    }
    ARGS_LOAD();
    TAG_LOAD();
    args();
    u32x4 GA[2][2];
#pragma unroll
    for (int mt = 0; mt < 2; ++mt) {
      const int hh = (l31 >> 2) & 1, reg = (l31 & 3) + 4 * (l31 >> 3);
      const int s2a = 2 * rg + (rgw >> 1);
      const int pp = 16 * mt + 8 * hh + (reg >> 1), cs = reg & 1;
      const bool sp = (pp == 0) && cs;
      const float ca = sp ? 0.5f : (float)pp * (1.0f / 64.0f);
      float cb = (float)(pp * s2a) * (1.0f / 2048.0f) - 0.25f * (float)cs;
      PIN(cb);
      cb = __builtin_fmaf(ca, (float)(32 * (rgw & 1) + 4 * h), sp ? 0.0f : cb);
      const float tw = 2.0f * cosr(8.0f * ca);
      float g[4][4];
#pragma unroll
      for (int e = 0; e < 4; ++e) {
        g[0][e] = cosr(__builtin_fmaf(ca, (float)e, cb));
        g[1][e] = cosr(__builtin_fmaf(ca, (float)(e + 8), cb));
        g[2][e] = __builtin_fmaf(tw, g[1][e], -g[0][e]);
        g[3][e] = __builtin_fmaf(tw, g[2][e], -g[1][e]);
      }
#pragma unroll
      for (int e = 0; e < 8; ++e) GA[mt][e >> 2][e & 3] = pk_h(g[e >> 1][2 * (e & 1)], g[e >> 1][2 * (e & 1) + 1]);
      PIN(GA[mt][0]); PIN(GA[mt][1]);
    }
    const int p = (tid & 255) >> 3, kdl = tid & 7;
    const float ff = p ? 1.0f : 0.0f;
    f16x2 k1 = h2(1.0f, -ff), k2 = h2(ff, 1.0f), k3[2], k4[2], k5[2];
#pragma unroll
    for (int sg = 0; sg < 2; ++sg) {
      const float ang = p ? 0.0f : (float)(2 * rg + sg) * (1.0f / 64.0f);
      const float c = cosr(ang), sn = sinr(ang);
      k3[sg] = h2(ff, c); k4[sg] = h2(-c, ff); k5[sg] = h2(sn, 0.0f);
      PIN(k3[sg]); PIN(k4[sg]); PIN(k5[sg]);
    }
    PIN(k1); PIN(k2);
    xaddr();
    unsigned sta, rra, gw[4], ga, spm, zo, zo1, zxo;
    u32x4 faug, xu;
    tag = get_tag();
    STAMP(2);
    f32x16 U = {};
    unsigned have = 0, po = LB + L_PROG + rgw * 4;
    PIN(po);
    sfor<0, 8>([&](auto ic) INL {
      constexpr int i = ic;
      if constexpr (i < 7) {
        wait_one<i + 1>(po, have);
        if constexpr (i == 0) STAMP(3);
        if constexpr (i == 6) STAMP(4);
#pragma unroll
        for (int u = 4 * i; u < 4 * i + 4; ++u) xread(u);
        SB();
      }
      if constexpr (i > 0) {
#pragma unroll
        for (int u = 4 * i - 4; u < 4 * i; ++u) U = mfma_bf(X[u], F[u], U);
        SB();
      }
      if constexpr (i == 3) {
        sta = LB + (unsigned)(32 * rgw + l31) * 4u, rra = LB + L_RS + (unsigned)(rgw * 32 + 4 * h) * 4u;
        ga = LB + L_GATH + (unsigned)(kdl * 128 + (((p >> 2) ^ kdl) * 16) + (p & 3) * 4);
#pragma unroll
        for (int q = 0; q < 4; ++q) {
          gw[q] = LB + L_GATH + (unsigned)(rgw * 4096 + l31 * 128 + (((4 * (q >> 1) + 2 * h + (q & 1)) ^ (l31 & 7)) * 16));
          PIN(gw[q]);
        }
        PIN(sta); PIN(rra); PIN(ga);
      }
      if constexpr (i == 4) {
        const bool sp = (j == 0) && (kdl == 0);
        spm = sp ? 0u : ~0u;
        zo = WS_Z + (unsigned)((2 * j * 16 + rg) * 4096 + (tid & 255) * 16), zo1 = zo + 65536u;
        zxo = sp ? WS_Z + (unsigned)((28 * 16 + rg) * 4096 + p * 128) : OOB;
        faug = u32x4{(j == 0 && lane == 0) ? 0x43e0u : 0u, 0u, 0u, 0u};
        xu = u32x4{0u, 0u, 0u, 0u};
        PIN(spm); PIN(zo); PIN(zo1); PIN(zxo); PIN(faug); PIN(xu);
      }
      if constexpr (i == 5) prog(5);
      SB();
    });
    PIN(U);
    __builtin_amdgcn_s_setprio(3);
    STAMP(5);
    wait_one<8>(po, have);
    STAMP(6);
    f32x4 rr[4];
    xu[0] = *(LAS unsigned*)(sta + L_XA);
#pragma unroll
    for (int q = 0; q < 4; ++q) rr[q] = *(LAS f32x4*)(rra + 32 * q);
    rstd = *(LAS float*)(sta + L_RS);
    SB();
    ms = xu[0];
    U = mfma_bf(xu, faug, U);
    u32x4 ub[2];
#pragma unroll
    for (int q = 0; q < 4; ++q) {
      ub[q >> 1][2 * (q & 1)] = pk_h(U[4 * q] * rr[q][0], U[4 * q + 1] * rr[q][1]);
      ub[q >> 1][2 * (q & 1) + 1] = pk_h(U[4 * q + 2] * rr[q][2], U[4 * q + 3] * rr[q][3]);
    }
    f32x16 za[2] = {};
#pragma unroll
    for (int t = 0; t < 2; ++t)
#pragma unroll
      for (int mt = 0; mt < 2; ++mt) za[mt] = mfma_h(GA[mt][t], ub[t], za[mt]);
#pragma unroll
    for (int q = 0; q < 4; ++q) {
      u32x4 v;
#pragma unroll
      for (int e = 0; e < 4; ++e) v[e] = pk_h2(pr(za[q >> 1], 8 * (q & 1) + 2 * e));
      lds128w(gw[q], v);
    }
    prog(6);
    STAMP(7);
    wait_prog(pa, 6, 0xf0);
    STAMP(8);
    f16x2 gv[16];
#pragma unroll
    for (int n = 0; n < 16; ++n) gv[n] = *(LAS f16x2*)(ga + (unsigned)((n >> 3) * 1024 + ((n >> 2) & 1) * 8192 + ((n >> 1) & 1) * 2048 + (n & 1) * 4096));
    SB();
    u32x4 zx;
#pragma unroll
    for (int gl = 0; gl < 2; ++gl) {
      u32x4 z;
#pragma unroll
      for (int sg = 0; sg < 2; ++sg) {
        const f16x2 t0 = gv[8 * gl + 4 * sg] + gv[8 * gl + 4 * sg + 1];
        f16x2 t1 = gv[8 * gl + 4 * sg + 2] + gv[8 * gl + 4 * sg + 3];
        if (gl == 0) {
          zx[sg] = __builtin_bit_cast(unsigned, t1);
          t1 = __builtin_bit_cast(f16x2, zx[sg] & spm);
        }
        z[sg] = __builtin_bit_cast(unsigned, hfma(t0, k1, t1.yx * k2));
        z[2 + sg] = __builtin_bit_cast(unsigned, hfma(t1.yy, k5[sg].yx, hfma(t0.yy, k5[sg], hfma(t0, k3[sg], t1.yx * k4[sg]))));
      }
      __builtin_amdgcn_raw_buffer_store_b128(z, rws, gl ? zo1 : zo, 0, 16);
      SB();
    }
#pragma unroll
    for (int sg = 0; sg < 2; ++sg) {
      const f16x2 t1 = __builtin_bit_cast(f16x2, (unsigned)zx[sg]);
      zx[sg] = __builtin_bit_cast(unsigned, t1 * k1);
      zx[2 + sg] = __builtin_bit_cast(unsigned, hfma(t1.yy, k5[sg], t1 * k3[sg]));
    }
    __builtin_amdgcn_raw_buffer_store_b128(zx, rws, zxo, 0, 16);
    SB();
    STAMP(9);
    __builtin_amdgcn_s_setprio(1);
    sfor<0, 14>(wdma);
    g1setup();
    WAITVM(14);
    flag_store(rws, WS_ZF + (unsigned)((rg * 14 + j) * 512 + rgw * 128), lane, tag);
    STAMP(1);
    dma_w2<1>(bE, 2048, vw2, vw2b, sw2, 0);
    dma_w2<2>(bE, 16384, vw2, vw2b, sw2, 14336);
    WAITVM(14);
    prog(9);
    wait_prog(pa, 9, 0xf0);
    dma_w2<2>(bE, 32768, vw2, vw2b, sw2, 3 * 14336);
  }
  STAMP(11);
  const unsigned gmask = grp ? 0xf0u : 0x0fu;

#define SEQ_U(i) (((i) + 6) % 28)
#define W2O(t) ((56 * ((t) / 14) + (t) % 14) * 14336)
  auto frag2 = [&](unsigned a) INL { return trfrag(a, a + 512); };
  f32x16 h0 = {}, h1 = {};
  const unsigned bG = bD + (grp ? 0u : L_C0);
  {
    constexpr int PD = 3, C0 = 12;
    u32x4 wf[28][2], ce[7][2], cg0, cg1;
    auto wload = [&](int u, int x) INL { wf[u][x] = frag2(w1a[(SEQ_U(u) ? 0 : 2) + x] + 2048u * SEQ_U(u)); };
#pragma unroll
    for (int u = 0; u < 2 * PD; ++u) wload(u >> 1, u & 1);
    const u32x4 one[2] = {{o0, o0, o0, o0}, {o1, o1, o1, o1}};
    const unsigned csa = LB + L_CS + (unsigned)(grp * 4096);
    f32x4 cs = {};
    unsigned b10, b11, pv;
    sfor<0, 28>([&](auto uc) INL {
      constexpr int u = uc;
      if constexpr (u == 3) {
        asm volatile("s_cmp_lg_u32 %0, 0\n\ts_cbranch_scc1 1f\n\ts_waitcnt vmcnt(0)\n1:\ts_waitcnt vmcnt(11)" :: "s"(grp) : "memory", "scc");
        prog(10);
        xres = __builtin_amdgcn_raw_buffer_load_b128(__builtin_amdgcn_make_buffer_rsrc(argp(ka, 0), 0, 2048 * 448 * 2, 0x00020000), oo, 0, 0);
        STAMP2(7, -1);
      }
      if constexpr (u == 13 - PD || u == 24) pv = *(volatile LAS unsigned*)pa;
      if constexpr (u == 14 - PD) {
        check_prog<10>(pv, pa, gmask);
        STAMP2(8, -1);
      }
      if constexpr (u == 25) {
        check_prog<11>(pv, pa, gmask);
        cg0 = lds128(csa + (unsigned)(l31 * 16));
        cg1 = lds128(csa + (unsigned)(l31 * 16 + 512));
        b10 = *(LAS unsigned short*)(LB + L_B1 + (unsigned)(grp * 128 + l31 * 2));
        b11 = *(LAS unsigned short*)(LB + L_B1 + (unsigned)(grp * 128 + l31 * 2 + 64));
        STAMP2(9, 0);
      }
#pragma unroll
      for (int x = 0; x < 2; ++x) {
        if (x) h1 = mfma_bf(wf[u][1], X[SEQ_U(u)], h1);
        else h0 = mfma_bf(wf[u][0], X[SEQ_U(u)], h0);
        if constexpr (u + PD < 28) wload(u + PD, x);
        if constexpr (u >= C0 && u < C0 + 7) ce[u - C0][x] = frag2((u == C0 ? (rgw ? w1a[4 + x] : w1a[2 + x]) : w1a[4 + x]) + 2048u * (u - C0));
        SB();
        if constexpr (u >= C0 + 2 && u < C0 + 9) { cs = mfma16(one[x], ce[u - C0 - 2][x], cs); SB(); }
      }
      if constexpr (u == C0 + 10) {
        const float hi = bf16r(cs[0]);
        *(LAS unsigned*)(csa + (unsigned)(lane * 16 + rgw * 4)) = pk_bf(hi, cs[0] - hi);
        WAITVM(1);
        prog(11);
      }
      if constexpr (u == 25) dma_w2<2>(bG, 0, vw2, vw2b, sw2, grp ? W2O(12) : W2O(5));
      if constexpr (u == 26) dma_w2<2>(bG, 4096, vw2, vw2b, sw2, grp ? W2O(14) : W2O(7));
      if constexpr (u == 27) dma_w2<1>(bG, 8192, vw2, vw2b, sw2, grp ? W2O(16) : W2O(9));
      SB();
    });
    prog(12);
    const unsigned mm = __builtin_amdgcn_perm(ms, ms, 0x01000100u);
    const u32x4 xaug = {h ? ms >> 16 : mm, mm, mm, mm};
    h0 = mfma_bf(u32x4{h ? b10 : cg0[0], h ? 0u : cg0[1], h ? 0u : cg0[2], h ? 0u : cg0[3]}, xaug, h0);
    h1 = mfma_bf(u32x4{h ? b11 : cg1[0], h ? 0u : cg1[1], h ? 0u : cg1[2], h ? 0u : cg1[3]}, xaug, h1);
    PIN(h0); PIN(h1);
  }
  __builtin_amdgcn_s_setprio(0);
  STAMP(12);
  {
    const float sig = bf_hi(ms), gc1 = -2.302208198f * rstd, gc2 = gc1 * 0.044715f * rstd * rstd;
    const unsigned ho = WS_H + (unsigned)((((2 * rg + (rgw >> 1)) * 112 + 56 * grp + 4 * j) * 2 + (rgw & 1)) * 1024 + lane * 16);
    sfor<0, 4>([&](auto tc) INL {
      constexpr int t = tc;
      const f32x16& hv = t < 2 ? h0 : h1;
      float g[8];
#define G8(expr) _Pragma("unroll") for (int e = 0; e < 8; ++e) { const float u = hv[8 * (t & 1) + e]; (void)u; g[e] = (expr); } SB();
      G8(u * u)
      G8(__builtin_fmaf(g[e], gc2, gc1))
      G8(g[e] * u)
      G8(__builtin_amdgcn_exp2f(g[e]))
      G8(__builtin_fmaf(g[e], sig, sig))
      G8(__builtin_amdgcn_rcpf(g[e]))
      G8(g[e] * u)
      const u32x4 o = {pk_bf(g[0], g[1]), pk_bf(g[2], g[3]), pk_bf(g[4], g[5]), pk_bf(g[6], g[7])};
      __builtin_amdgcn_raw_buffer_store_b128(o, rws, ho + t * 2048, 0, 16);
      SB();
      if constexpr (t == 0) {
        wait_prog(pa, 12, grp ? 0xffu : 0x0fu);
        dma_w2<1>(grp ? bE : bF + L_C0, 0, vw2, vw2b, sw2, grp ? W2O(18) : W2O(10));
        dma_w2<1>(grp ? bF : bF + L_C0, 2048, vw2, vw2b, sw2, grp ? W2O(17) : W2O(11));
        STAMP(13);
      }
    });
  }
  STAMP(14);

  const rsrc_t rout = __builtin_amdgcn_make_buffer_rsrc(argp(ka, 6), 0, 2048 * 448 * 2, 0x00020000);
  const unsigned hfo = WS_HF + (unsigned)((rg * 14 + j) * 1024 + w * 128);
  f32x16 acc[2][2] = {};
  if (grp == 0) {
    const unsigned hpo = WS_HF + (unsigned)((rg * 14 + ((14 * rgw) >> 2) + ((lane >> 1) & 3)) * 1024 + (2 * (s2 & 1) + (lane & 1)) * 128);
    const unsigned hi = WS_H + (unsigned)((s2 * 112 + 14 * rgw) * 2048 + lane * 16);
    const unsigned w2l = (unsigned)((4 * h + tq) * 128 + 64 * (tq >> 1) + 32 * tg + 8 * tp);
#define SLOT_R(t) ((t) < 19 ? (t) : (t) - 19)
    auto slot_b = [&](int t) INL { const int r = SLOT_R(t); return r < 5 || r == 18 ? bE : r < 10 ? bG : (r < 12 || r == 17) ? bF : bD; };
#define SLOT_O(t) (SLOT_R(t) == 0 ? 2048 : SLOT_R(t) < 3 ? 14336 + 2048 * SLOT_R(t) : SLOT_R(t) < 5 ? 26624 + 2048 * SLOT_R(t) : \
                   SLOT_R(t) < 10 ? 2048 * (SLOT_R(t) - 5) : SLOT_R(t) < 12 ? L_C0 + 2048 * (SLOT_R(t) - 10) : \
                   SLOT_R(t) < 17 ? 2048 * (SLOT_R(t) - 12) : SLOT_R(t) == 17 ? 2048 : 0)
    constexpr int HD = 8;
    u32x4 H[28][2], W[28][2];
    auto hload = [&](int t) INL {
#pragma unroll
      for (int sh = 0; sh < 2; ++sh) H[t][sh] = __builtin_amdgcn_raw_buffer_load_b128(rws, hi + sh * 1024, (56 * (t / 14) + t % 14) * 2048, 16);
    };
    auto wload = [&](int t) INL {
#pragma unroll
      for (int x = 0; x < 2; ++x) {
        const unsigned a = slot_b(t) + (w2l ^ (64u * x)) + SLOT_O(t);
        W[t][x] = trfrag(a, a + 1024);
      }
    };
#define FLAG_LOAD(v, o) asm volatile("buffer_load_dwordx2 %0, %1, %2, 0 offen offset:" #o " sc1" : "=v"(v) : "v"(hpo), "s"(sws) : "memory")
#define FLAG_BAD(v) (__builtin_amdgcn_ballot_w64(v[0] != tag[0] || v[1] != tag[1]) != 0)
    WAITVM(0);
    STAMP(15);
    flag_store(rws, hfo, lane, tag);
#pragma unroll
    for (int t = 5; t < 9; ++t) wload(t);
    wait_prog(pa, 11, 16u << rgw);
#pragma unroll
    for (int t = 0; t < 5; ++t) wload(t);
    dma_w2<2>(bG, 0, vw2, vw2b, sw2, W2O(24));
    dma_w2<2>(bG, 4096, vw2, vw2b, sw2, W2O(26));
    u32x2 f0, f1;
    FLAG_LOAD(f0, 0);
#pragma unroll
    for (int t = 0; t < 9; ++t) { PIN(W[t][0]); PIN(W[t][1]); }
    dma_w2<1>(bE, 2048, vw2, vw2b, sw2, W2O(19));
    dma_w2<2>(bE, 16384, vw2, vw2b, sw2, W2O(20));
    dma_w2<2>(bE, 32768, vw2, vw2b, sw2, W2O(22));
    FLAG_LOAD(f1, 0);
    STAMP(16);
    asm volatile("s_waitcnt vmcnt(11)" : "+v"(f0) :: "memory");
    STAMP(1);
    if (FLAG_BAD(f0)) {
      asm volatile("s_waitcnt vmcnt(0)" : "+v"(f1) :: "memory");
      STAMP(2);
      if (FLAG_BAD(f1)) poll(sws, hpo, tag);
    }
    STAMP(17);
#pragma unroll
    for (int t = 0; t < HD; ++t) hload(t);
    __builtin_amdgcn_s_setprio(2);
    SB();
    STAMP(18);
    sfor<0, 28>([&](auto tc) INL {
      constexpr int t = tc;
      if constexpr (t == 10) wait_prog(pa, 14, 16u << rgw);
      if constexpr (t + 2 < 28 && t + 2 >= 9) wload(t + 2);
      acc[0][0] = mfma_bf(W[t][0], H[t][0], acc[0][0]);
      acc[0][1] = mfma_bf(W[t][0], H[t][1], acc[0][1]);
      SB();
      if constexpr (t == 0) asm volatile("" :: "v"(f1));
      if constexpr (t == 2) FLAG_LOAD(f0, 512);
      if constexpr (t == 9) {
        asm volatile("s_waitcnt vmcnt(8)" : "+v"(f0) :: "memory");
        PIN(acc[0][0]); PIN(acc[0][1]); PIN(acc[1][0]); PIN(acc[1][1]);
        STAMP(3);
        if (FLAG_BAD(f0)) poll(sws, hpo + 512u, tag);
        STAMP(19);
        hload(14); hload(15); hload(16);
      }
      if constexpr (t + HD < 28 && (t < 6 || t >= 9)) hload(t + HD);
      SB();
      acc[1][0] = mfma_bf(W[t][1], H[t][0], acc[1][0]);
      acc[1][1] = mfma_bf(W[t][1], H[t][1], acc[1][1]);
      SB();
      if constexpr (t == 17) prog(13);
      if constexpr (t == 13) STAMP(20);
    });
    __builtin_amdgcn_s_setprio(0);
    STAMP(21);
  } else {
    WAITVM(0);
    STAMP(15);
    flag_store(rws, hfo, lane, tag);
    prog(14);
    STAMP(16);
    const int rho = rgw & 1, kap = rgw >> 1;
    u32x4 I[15][2];
#pragma unroll
    for (int x = 0; x < 2; ++x) {
      const float d = (float)(64 * cg + 32 * x + l31), dl = d * (1.0f / 448.0f);
      const float tw = 2.0f * cosr(rev448(8.0f * d));
      const float k0 = (float)(112 * kap + 4 * h);
      const float a0 = rev448(k0 * d), a1 = rev448((k0 + 8.0f) * d);
      float f0[8], f1[8];
#pragma unroll
      for (int e = 0; e < 4; ++e) {
        const float b0 = __builtin_fmaf((float)e, dl, a0), b1 = __builtin_fmaf((float)e, dl, a1);
        f0[e] = cosr(b0); f0[4 + e] = sinr(-b0);
        f1[e] = cosr(b1); f1[4 + e] = sinr(-b1);
      }
#pragma unroll
      for (int i = 0; i < 15; ++i) {
#pragma unroll
        for (int e = 0; e < 4; ++e) I[i][x][e] = pk_bf(f0[2 * e], f0[2 * e + 1]);
#pragma unroll
        for (int e = 0; e < 8; ++e) {
          const float n = __builtin_fmaf(tw, f1[e], -f0[e]);
          f0[e] = f1[e];
          f1[e] = n;
        }
        PIN(I[i][x]);
      }
      if (x == 0) STAMP(17);
    }
    u32x4 GC[2][2];
#pragma unroll
    for (int sh = 0; sh < 2; ++sh) {
      const float sf = (float)(32 * (32 * sh + l31) + s2) * (1.0f / 2048.0f);
#pragma unroll
      for (int t = 0; t < 2; ++t)
#pragma unroll
        for (int e = 0; e < 4; ++e) {
          const int p = 16 * rho + 8 * t + 4 * (e >> 1) + 2 * h + (e & 1);
          const float a = (float)p * sf;
          float sv = sinr(a);
          if (t == 0 && e == 0) sv = (p == 0) ? ((l31 & 1) ? -1.0f : 1.0f) : sv;
          GC[sh][t][e] = pk_bf(cosr(a), sv);
        }
      PIN(GC[sh][0]); PIN(GC[sh][1]);
    }
    unsigned ai = WS_A + (unsigned)(((s2 * 2 + rho) * 29 + 14 * kap) * 1024 + (l31 >> 3) * 256 + h * 128 + (l31 & 7) * 16);
    PIN(ai);
    STAMP(18);
    poll(sws, WS_AF + (unsigned)(((4 * rho + (lane & 3)) * 4 + 2 * kap + ((lane >> 2) & 1)) * 128), tag);
    STAMP(19);
    u32x4 A[15];
#pragma unroll
    for (int i = 0; i < 15; ++i) A[i] = __builtin_amdgcn_raw_buffer_load_b128(rws, (i == 14 && kap == 0) ? OOB : ai, i * 1024, 16);
    __builtin_amdgcn_s_setprio(1);
    SB();
    f32x16 T[2] = {};
#pragma unroll
    for (int i = 0; i < 15; ++i) {
      T[0] = mfma_bf(A[i], I[i][0], T[0]);
      T[1] = mfma_bf(A[i], I[i][1], T[1]);
    }
    STAMP(20);
#pragma unroll
    for (int x = 0; x < 2; ++x)
#pragma unroll
      for (int t = 0; t < 2; ++t) {
        u32x4 tf;
#pragma unroll
        for (int e = 0; e < 4; ++e) tf[e] = pk_bf2(pr(T[x], 8 * t + 2 * e));
        acc[x][0] = mfma_bf(tf, GC[0][t], acc[x][0]);
        acc[x][1] = mfma_bf(tf, GC[1][t], acc[x][1]);
      }
    __builtin_amdgcn_s_setprio(0);
    wait_prog(pa, 13, 1u << rgw);
    STAMP(21);
  }
  {
    const unsigned pb = bG + (unsigned)(l31 * 128 + h * 8);
#pragma unroll
    for (int x = 0; x < 2; ++x)
#pragma unroll
      for (int sh = 0; sh < 2; ++sh)
#pragma unroll
        for (int q = 0; q < 4; ++q) {
          const u32x2 v = {pk_bf2(pr(acc[x][sh], 4 * q)), pk_bf2(pr(acc[x][sh], 4 * q + 2))};
          *(LAS u32x2*)(pb + (unsigned)(sh * 4096 + (((4 * x + q) ^ (l31 & 7)) * 16))) = v;
        }
  }
  {
    const unsigned ra = LB + (unsigned)((tid >> 3) * 128 + (((tid & 7) ^ ((tid >> 3) & 7)) * 16));
    const u32x4 b2v = lds128(LB + L_B2 + (unsigned)((tid & 7) * 16));
    float r[8];
#pragma unroll
    for (int e = 0; e < 4; ++e) {
      r[2 * e] = bf_lo(xres[e]) + bf_lo(b2v[e]);
      r[2 * e + 1] = bf_hi(xres[e]) + bf_hi(b2v[e]);
      PIN(r[2 * e]); PIN(r[2 * e + 1]);
    }
    unsigned slo = 0x00003f80u, shi = 0x3f800000u;
    asm volatile("" : "+s"(slo), "+s"(shi));
    STAMP(22);
    LBAR();
    STAMP(10);
    u32x4 v[8];
#pragma unroll
    for (int k = 0; k < 8; ++k) v[k] = lds128(ra + 16384 + (k & 3) * 10240 + (k < 4 ? L_C0 : 0));
    SB();
#pragma unroll
    for (int k = 0; k < 8; ++k)
#pragma unroll
      for (int e = 0; e < 4; ++e) {
        r[2 * e] = dot2(slo, v[k][e], r[2 * e]);
        r[2 * e + 1] = dot2(shi, v[k][e], r[2 * e + 1]);
      }
    const u32x4 o = {pk_bf(r[0], r[1]), pk_bf(r[2], r[3]), pk_bf(r[4], r[5]), pk_bf(r[6], r[7])};
    __builtin_amdgcn_raw_buffer_store_b128(o, rout, oo, 0, 16);
  }
  if (blockIdx.x == 0 && tid == 0) __builtin_amdgcn_raw_buffer_store_b64(tag, rws, WS_E, 0, 16);
  STAMP(23);
  STAMP_OUT();
}

extern "C" void kernel_launch(void* const* d_in, const int* in_sizes, int n_in, void* d_out, int out_size,
                              void* d_ws, size_t ws_size, hipStream_t stream) {
  fused_kernel<<<256, 512, 0, stream>>>(d_in[0], d_in[4], d_ws, d_in[6], d_in[5], d_in[7], d_out, d_in[1], d_in[2]);
}
```

```cpp
#include <hip/hip_runtime.h>
#include <stdint.h>
#include <type_traits>
#pragma clang diagnostic ignored "-Wint-to-pointer-cast"
#pragma clang diagnostic ignored "-Winline-asm"

#define DEV __device__ __forceinline__
#define INL __attribute__((always_inline))
#if defined(__HIP_DEVICE_COMPILE__)
#define NOPK __attribute__((target("no-packed-fp32-ops")))
#else
#define NOPK
#endif
#define LAS __attribute__((address_space(3)))
typedef __attribute__((ext_vector_type(4))) short s16x4;
typedef __attribute__((ext_vector_type(8))) short s16x8;
typedef __attribute__((ext_vector_type(8))) __bf16 bf16x8;
typedef __attribute__((ext_vector_type(2))) __bf16 bf16x2;
typedef __attribute__((ext_vector_type(8))) _Float16 f16x8;
typedef __attribute__((ext_vector_type(2))) _Float16 f16x2;
typedef __attribute__((ext_vector_type(2))) float f32x2;
typedef __attribute__((ext_vector_type(4))) float f32x4;
typedef __attribute__((ext_vector_type(16))) float f32x16;
typedef __attribute__((ext_vector_type(2))) unsigned u32x2;
typedef __attribute__((ext_vector_type(4))) unsigned u32x4;
typedef __attribute__((ext_vector_type(16))) unsigned u32x16;
typedef __amdgpu_buffer_rsrc_t rsrc_t;

constexpr unsigned WS_E = 0;
constexpr unsigned WS_ZF = 4096;
constexpr unsigned WS_HF = WS_ZF + 224 * 512;
constexpr unsigned WS_AF = WS_HF + 224 * 1024;
constexpr unsigned WS_Z = 393216;
constexpr unsigned WS_A = WS_Z + 29 * 16 * 4096;
constexpr unsigned WS_H = WS_A + 32 * 2 * 29 * 1024;
constexpr unsigned WS_END = WS_H + 32 * 112 * 2048;
constexpr unsigned OOB = 0x80000000u;
constexpr int SPIN_CAP = 1 << 16;
constexpr int HD = 5;
constexpr int TFL = 6, TCH = 11;
constexpr int TA = 10;
constexpr int TYY = 25;
constexpr int F0N = 3;
#define BDELAY 24
#define CODE_BYTES 33152

constexpr unsigned L_PROG = 0;
constexpr unsigned L_RS = 512;
constexpr unsigned L_XA = 1024;
constexpr unsigned L_B1 = 1536, L_B2 = 1792;
constexpr unsigned L_C0 = 57344;
constexpr unsigned L_E = 114688;
constexpr unsigned L_GATH = L_E + 32768;
constexpr unsigned L_CS = L_E + 4096;
DEV constexpr unsigned xblk(int kc) { return kc < 4 ? 2048u + 16384u * kc : L_E + 16384u * (kc - 4); }

#define WAITBAR(n) asm volatile("s_waitcnt vmcnt(" #n ") lgkmcnt(0)\n\ts_barrier" ::: "memory")
#define LBAR() asm volatile("s_waitcnt lgkmcnt(0)\n\ts_barrier" ::: "memory")
#define PIN(x) asm volatile("" : "+v"(x))
#define SB() __builtin_amdgcn_sched_barrier(0)
#define WAITVM(n) asm volatile("s_waitcnt vmcnt(" #n ")" ::: "memory")

DEV u32x4 mk_srd(const void* p, unsigned bytes) {
  uint64_t a = (uint64_t)p;
  u32x4 r = {(unsigned)a, (unsigned)(a >> 32) & 0xffffu, bytes, 0x00020000u};
  return r;
}
DEV u32x4 lds128(unsigned a) { return *(LAS u32x4*)a; }
DEV void lds128w(unsigned a, u32x4 v) { *(LAS u32x4*)a = v; }
DEV u32x4 trfrag(unsigned a0, unsigned a1) {
  s16x4 lo = __builtin_amdgcn_ds_read_tr16_b64_v4i16((LAS s16x4*)a0);
  s16x4 hi = __builtin_amdgcn_ds_read_tr16_b64_v4i16((LAS s16x4*)a1);
  return __builtin_bit_cast(u32x4, __builtin_shufflevector(lo, hi, 0, 1, 2, 3, 4, 5, 6, 7));
}
DEV f32x16 mfma_bf(u32x4 a, u32x4 b, f32x16 c) {
  return __builtin_amdgcn_mfma_f32_32x32x16_bf16(__builtin_bit_cast(bf16x8, a), __builtin_bit_cast(bf16x8, b), c, 0, 0, 0);
}
DEV f32x16 mfma_h(u32x4 a, u32x4 b, f32x16 c) {
  return __builtin_amdgcn_mfma_f32_32x32x16_f16(__builtin_bit_cast(f16x8, a), __builtin_bit_cast(f16x8, b), c, 0, 0, 0);
}
DEV unsigned pk_bf(float lo, float hi) {
  f32x2 p = {lo, hi};
  return __builtin_bit_cast(unsigned, __builtin_convertvector(p, bf16x2));
}
DEV unsigned pk_h(float lo, float hi) {
  f32x2 p = {lo, hi};
  return __builtin_bit_cast(unsigned, __builtin_convertvector(p, f16x2));
}
DEV unsigned pk_bf2(f32x2 p) { return __builtin_bit_cast(unsigned, __builtin_convertvector(p, bf16x2)); }
DEV unsigned pk_h2(f32x2 p) { return __builtin_bit_cast(unsigned, __builtin_convertvector(p, f16x2)); }
DEV f32x2 pr(const f32x16& v, int r) { return f32x2{v[r], v[r + 1]}; }
DEV f32x2 fma2(f32x2 a, f32x2 b, f32x2 c) { return __builtin_elementwise_fma(a, b, c); }
DEV f32x2 bc(float a) { return f32x2{a, a}; }
DEV float bf_lo(unsigned v) { return __builtin_bit_cast(float, v << 16); }
DEV float bf_hi(unsigned v) { return __builtin_bit_cast(float, v & 0xffff0000u); }
DEV float bf16r(float x) { return bf_lo(pk_bf(x, 0.f)); }
DEV float dot2(unsigned a, unsigned b, float c) {
  return __builtin_amdgcn_fdot2_f32_bf16(__builtin_bit_cast(bf16x2, a), __builtin_bit_cast(bf16x2, b), c, false);
}
DEV float cosr(float rev) { return __builtin_amdgcn_cosf(rev); }
DEV float sinr(float rev) { return __builtin_amdgcn_sinf(rev); }
DEV float rev448(float n) {
  float q = __builtin_floorf(n * (1.0f / 448.0f));
  return __builtin_fmaf(q, -448.0f, n) * (1.0f / 448.0f);
}
DEV void poll(u32x4 srd, unsigned voff, u32x2 tag) {
#pragma unroll 1
  for (int n = 0; n < SPIN_CAP; ++n) {
    u32x2 v;
    asm volatile("buffer_load_dwordx2 %0, %1, %2, 0 offen sc1\n\ts_waitcnt vmcnt(0)"
                 : "=&v"(v) : "v"(voff), "s"(srd) : "memory");
    if (__builtin_amdgcn_ballot_w64(v[0] != tag[0] || v[1] != tag[1]) == 0) break;
    __builtin_amdgcn_s_sleep(8);
  }
}
template <int A, int B, class Fn> DEV void sfor(Fn&& f) {
  if constexpr (A < B) { f(std::integral_constant<int, A>{}); sfor<A + 1, B>(f); }
}

DEV void dma_x(unsigned lds, unsigned lit, unsigned v0, unsigned v1, unsigned v2, unsigned v3, u32x4 srd, unsigned& col) {
  unsigned t;
  asm volatile("s_add_u32 m0, %2, %3\n\ts_add_u32 %1, %0, 128\n\t"
               "buffer_load_dwordx4 %4, %8, %0 offen lds\n\t"
               "buffer_load_dwordx4 %5, %8, %0 offen offset:1024 lds\n\t"
               "buffer_load_dwordx4 %6, %8, %0 offen offset:2048 lds\n\t"
               "buffer_load_dwordx4 %7, %8, %0 offen offset:3072 lds\n\t"
               "s_sub_u32 %0, %0, 768\n\ts_min_u32 %0, %0, %1"
               : "+s"(col), "=&s"(t) : "s"(lds), "n"(lit), "v"(v0), "v"(v1), "v"(v2), "v"(v3), "s"(srd) : "memory", "scc");
}
DEV void dma_w1(unsigned lds, unsigned lit, unsigned v, u32x4 srd, unsigned& row) {
  unsigned t;
  asm volatile("s_add_u32 m0, %2, %3\n\ts_add_u32 %1, %0, 114688\n\t"
               "buffer_load_dwordx4 %4, %5, %0 offen lds\n\t"
               "s_sub_u32 %0, %0, 1490944\n\ts_min_u32 %0, %0, %1"
               : "+s"(row), "=&s"(t) : "s"(lds), "n"(lit), "v"(v), "s"(srd) : "memory", "scc");
}
template <int N> DEV void dma_w2(unsigned lds, unsigned lit, unsigned v0, unsigned v1, u32x4 srd, unsigned so) {
  asm volatile("s_add_u32 m0, %1, %2\n\ts_nop 0\n\t"
               "buffer_load_dwordx4 %3, %5, %0 offen lds\n\t"
               "buffer_load_dwordx4 %4, %5, %0 offen offset:1024 lds\n\t"
               ".if %6 > 1\n\ts_add_u32 %0, %0, 12288\n\t"
               "buffer_load_dwordx4 %3, %5, %0 offen offset:2048 lds\n\t"
               "buffer_load_dwordx4 %4, %5, %0 offen offset:3072 lds\n\t.endif"
               : "+s"(so) : "s"(lds), "n"(lit), "v"(v0), "v"(v1), "s"(srd), "n"(N) : "memory", "scc");
}
DEV void dma_w2_if(int on, unsigned lds, unsigned lit, unsigned v0, unsigned v1, u32x4 srd, unsigned so) {
  asm volatile("s_cmp_eq_u32 %6, 0\n\ts_cbranch_scc1 1f\n\ts_add_u32 m0, %1, %2\n\ts_nop 0\n\t"
               "buffer_load_dwordx4 %3, %5, %0 offen lds\n\t"
               "buffer_load_dwordx4 %4, %5, %0 offen offset:1024 lds\n1:"
               :: "s"(so), "s"(lds), "n"(lit), "v"(v0), "v"(v1), "s"(srd), "s"(on) : "memory", "scc");
}
DEV void touch(unsigned lds, unsigned voff, u32x4 srd) {
  asm volatile("s_mov_b32 m0, %0\n\ts_nop 0\n\tbuffer_load_dword %1, %2, 0 offen lds" :: "s"(lds), "v"(voff), "s"(srd) : "memory");
}
DEV uint64_t landed() {
  uint64_t e;
  asm volatile("s_waitcnt lgkmcnt(0)\n\ts_mov_b64 %0, s[100:101]" : "=s"(e));
  return e;
}
DEV u32x2 get_tag() {
  uint64_t e = landed();
  e = e * 6364136223846793005ull + 1442695040888963407ull;
  asm volatile("" : "+s"(e));
  return u32x2{(unsigned)e, (unsigned)(e >> 32)};
}
DEV void* argp(const u32x16& k, int i) { return (void*)(((uint64_t)k[2 * i + 1] << 32) | k[2 * i]); }
template <int NEED> DEV void wait_prog_(unsigned a, unsigned mask) {
  unsigned v, n, t;
  asm volatile("s_movk_i32 %1, 0x7fff\n"
               "1:\tds_read_b32 %0, %3\n\ts_sub_u32 %1, %1, 1\n\ts_waitcnt lgkmcnt(0)\n\ts_cbranch_scc1 2f\n\t"
               "v_cmp_gt_u32 vcc, %5, %0\n\ts_and_b32 %2, vcc_lo, %4\n\ts_cbranch_scc1 1b\n2:"
               : "=&v"(v), "=&s"(n), "=&s"(t) : "v"(a), "s"(mask), "n"(NEED) : "memory", "scc", "vcc");
}
#define wait_prog(a, need, mask) wait_prog_<need>(a, mask)
template <int NEED> DEV void check_prog(unsigned late, unsigned a, unsigned mask) {
  unsigned v, n, t;
  asm volatile("s_and_b32 %2, %6, %4\n\ts_cbranch_scc0 2f\n\ts_movk_i32 %1, 0x7fff\n"
               "1:\tds_read_b32 %0, %3\n\ts_sub_u32 %1, %1, 1\n\ts_waitcnt lgkmcnt(0)\n\ts_cbranch_scc1 2f\n\t"
               "v_cmp_gt_u32 vcc, %5, %0\n\ts_and_b32 %2, vcc_lo, %4\n\ts_cbranch_scc1 1b\n2:"
               : "=&v"(v), "=&s"(n), "=&s"(t) : "v"(a), "s"(mask), "n"(NEED), "s"(late) : "memory", "scc", "vcc");
}
template <int NEED> DEV void wait_one(unsigned a, unsigned& have) {
  unsigned v, n;
  asm volatile("s_cmp_ge_u32 %2, %4\n\ts_cbranch_scc1 2f\n\ts_movk_i32 %1, 0x7fff\n"
               "1:\tds_read_b32 %0, %3\n\ts_sub_u32 %1, %1, 1\n\ts_waitcnt lgkmcnt(0)\n\ts_cbranch_scc1 2f\n\t"
               "v_readfirstlane_b32 %2, %0\n\ts_cmp_lt_u32 %2, %4\n\ts_cbranch_scc1 1b\n2:"
               : "=&v"(v), "=&s"(n), "+s"(have) : "v"(a), "n"(NEED) : "memory", "scc");
}
DEV void flag_store(rsrc_t r, unsigned off, int lane, u32x2 tag) {
  __builtin_amdgcn_raw_buffer_store_b128(u32x4{tag[0], tag[1], tag[0], tag[1]}, r, lane < 8 ? off + (unsigned)lane * 16u : OOB, 0, 16);
}
DEV f32x4 mfma4(unsigned a0, unsigned a1, unsigned b0, unsigned b1, f32x4 c) {
  return __builtin_amdgcn_mfma_f32_4x4x4bf16_1k(__builtin_bit_cast(s16x4, u32x2{a0, a1}), __builtin_bit_cast(s16x4, u32x2{b0, b1}), c, 0, 0, 0);
}
DEV float half_sum(float v) {
  const unsigned u = __builtin_bit_cast(unsigned, v);
  const auto r = __builtin_amdgcn_permlane32_swap(u, u, false, false);
  return __builtin_bit_cast(float, (unsigned)r[0]) + __builtin_bit_cast(float, (unsigned)r[1]);
}
DEV f16x2 h2(float a, float b) { return f16x2{(_Float16)a, (_Float16)b}; }
DEV f16x2 hfma(f16x2 a, f16x2 b, f16x2 c) { return __builtin_elementwise_fma(a, b, c); }

__global__ void NOPK __launch_bounds__(512, 2)
fused_kernel(const void* a_x, const void* a_w1, void* a_ws, const void* a_w2, const void* a_b1, const void* a_b2,
             void* a_out, const void* a_wr, const void* a_wi) {
  __shared__ __attribute__((aligned(1024))) char smem[163840];
  const unsigned LB = (unsigned)(uintptr_t)(LAS char*)smem;
  const int tid = threadIdx.x;
  const int lane = tid & 63;
  const int w = __builtin_amdgcn_readfirstlane(tid >> 6);
  const int l31 = lane & 31, h = lane >> 5;
  const int xcd = blockIdx.x & 7, loc = blockIdx.x >> 3;
  u32x16 ka;
  const auto kseg = __builtin_amdgcn_kernarg_segment_ptr();
  asm volatile("s_cmp_lg_u32 %1, 0\n\ts_cbranch_scc1 1f\n\ts_load_dwordx2 s[100:101], %0, 0\n\ts_load_dwordx2 s[98:99], %0, 64\n1:"
               :: "s"(kseg), "s"(loc < 28 ? w >> 2 : 0) : "scc", "s98", "s99", "s100", "s101");
#define ARGS_LOAD() asm volatile("s_load_dwordx16 %0, %1, 0\n\ts_waitcnt lgkmcnt(0)" : "=&s"(ka) : "s"(kseg) : "memory")
#define TAG_LOAD() asm volatile("s_load_dwordx2 s[100:101], %0, 0" :: "s"(argp(ka, 2)) : "s100", "s101")
  if (w == 4)
    touch(LB + 256, (unsigned)(loc + 32 * lane) * 128u, mk_srd((const void*)__builtin_amdgcn_s_getpc(), CODE_BYTES));

  if (__builtin_expect(loc >= 28, 0)) {
    const int pq = xcd, qt = loc - 28;
    const int ch = tid & 7, rw = tid >> 3;
    unsigned wo[4];
#pragma unroll
    for (int i = 0; i < 4; ++i) {
      const int idx = rw + 64 * (i & 1), p = 4 * pq + (idx & 3), k2 = idx >> 2;
      const int k = (i >> 1) ? (p ? ((2048 - p + 64 * k2) & 2047) : (32 + 64 * k2)) : (p + 64 * k2);
      wo[i] = (ch < 7 || qt == 3) ? (unsigned)(k * 225 + 56 * qt + (ch < 7 ? 8 * ch : 49)) * 2u : OOB;
      PIN(wo[i]);
    }
    ARGS_LOAD();
    uint64_t pwi;
    asm volatile("s_mov_b64 %0, s[98:99]" : "=s"(pwi));
    void* pws = argp(ka, 2);
    const u32x4 sws = mk_srd(pws, WS_END);
    const rsrc_t rws = __builtin_amdgcn_make_buffer_rsrc(pws, 0, WS_END, 0x00020000);
    const rsrc_t rwr = __builtin_amdgcn_make_buffer_rsrc(argp(ka, 7), 0, 2048 * 225 * 2, 0x00020000);
    const rsrc_t rwi = __builtin_amdgcn_make_buffer_rsrc((void*)pwi, 0, 2048 * 225 * 2, 0x00020000);
    {
      const unsigned n = (unsigned)(((qt * 8 + xcd) * 8 + w) * 49 + lane);
      touch(LB + 81920u, lane < 49 ? n * 128u : OOB, mk_srd(argp(ka, 1), 448 * 1792 * 2));
    }
    __builtin_amdgcn_s_sleep(BDELAY);
    SB();
    u32x4 wv[8];
#pragma unroll
    for (int i = 0; i < 4; ++i) {
      wv[i] = __builtin_amdgcn_raw_buffer_load_b128(rwr, wo[i], 0, 2);
      wv[4 + i] = __builtin_amdgcn_raw_buffer_load_b128(rwi, wo[i], 0, 2);
    }
    TAG_LOAD();
    SB();
    const bool active = (w < 7) || (qt == 3);
    const int g = 7 * qt + w;
    const int kdl = lane & 7, pl = (lane >> 3) & 3;
    const int p = 4 * pq + pl, kd = 8 * g + kdl;
    u32x4 FR[4], FI[4], IR[4], II[4];
#pragma unroll
    for (int t = 0; t < 4; ++t) {
      float c[4], sn[4];
#pragma unroll
      for (int e = 0; e < 4; ++e) {
        const float a = (float)(l31 * (8 * t + 4 * h + e)) * (1.0f / 32.0f);
        c[e] = cosr(a);
        sn[e] = sinr(a);
        FR[t][e] = pk_h(c[e], sn[e]);
        FI[t][e] = pk_h(-sn[e], c[e]);
      }
      IR[t] = u32x4{pk_bf(c[0], c[1]), pk_bf(c[2], c[3]), pk_bf(-sn[0], -sn[1]), pk_bf(-sn[2], -sn[3])};
      II[t] = u32x4{pk_bf(sn[0], sn[1]), pk_bf(sn[2], sn[3]), pk_bf(c[0], c[1]), pk_bf(c[2], c[3])};
      PIN(FR[t]); PIN(FI[t]); PIN(IR[t]); PIN(II[t]);
    }
#pragma unroll
    for (int i = 0; i < 8; ++i) lds128w(LB + (unsigned)(((i >> 1) * 128 + rw + 64 * (i & 1)) * 144 + ch * 16), wv[i]);
    const u32x2 tag = get_tag();
    LBAR();
    if (active) {
      f32x2 wpr[8], wpi[8], wmr[8], wmi[8];
      const float wsc = ((kd == 0 || kd == 224) ? 1.0f : 2.0f) / (2048.0f * 448.0f);
      const unsigned wa = LB + (unsigned)((16 * h + pl) * 144 + (w < 7 ? 8 * w + kdl : 63) * 2);
#pragma unroll
      for (int r = 0; r < 16; ++r) {
        const unsigned o = (unsigned)(((r & 3) + 8 * (r >> 2)) * 4 * 144);
        wpr[r >> 1][r & 1] = bf_lo(*(LAS unsigned short*)(wa + o));
        wmr[r >> 1][r & 1] = bf_lo(*(LAS unsigned short*)(wa + o + 128 * 144));
        wpi[r >> 1][r & 1] = bf_lo(*(LAS unsigned short*)(wa + o + 256 * 144));
        wmi[r >> 1][r & 1] = bf_lo(*(LAS unsigned short*)(wa + o + 384 * 144));
      }
      float ff = pq ? 1.0f : 0.0f;
      float cph = pq ? 1.0f : cosr((float)l31 * (1.0f / 64.0f));
      float sph = pq ? 0.0f : sinr((float)l31 * (1.0f / 64.0f));
      PIN(ff); PIN(cph); PIN(sph);
#pragma unroll
      for (int r = 0; r < 8; ++r) {
        wpr[r] *= bc(wsc); wpi[r] *= bc(wsc); wmr[r] *= bc(wsc); wmi[r] *= bc(wsc);
        PIN(wpr[r]); PIN(wpi[r]); PIN(wmr[r]); PIN(wmi[r]);
      }
      unsigned zo = (kd > 224) ? OOB : WS_Z + (unsigned)((g * 16 + 2 * h) * 4096 + p * 128 + kdl * 16);
      unsigned ao = WS_A + (unsigned)((((lane >> 4) * 2 + (pq >> 2)) * 29 + g) * 1024 + (pq & 3) * 256 + (lane & 15) * 16);
      unsigned tw = LB + 90112u + (unsigned)(w * 8192 + l31 * 256);
      unsigned tr = LB + 90112u + (unsigned)(w * 8192 + (lane >> 4) * 256);
      PIN(zo); PIN(ao); PIN(tw); PIN(tr);
      const int jz = (g == 28) ? 0 : (g >> 1);
      poll(sws, WS_ZF + (unsigned)(((lane & 15) * 14 + jz) * 512 + (pq >> 1) * 128), tag);

      u32x4 z[8];
#pragma unroll
      for (int i = 0; i < 8; ++i)
        z[i] = __builtin_amdgcn_raw_buffer_load_b128(rws, zo, (4 * (i >> 1) + (i & 1)) * 4096, 16);
      SB();
      f32x16 xpr = {}, xpi = {}, xmr = {}, xmi = {};
#pragma unroll
      for (int t = 0; t < 4; ++t) {
        const u32x4 zp = {z[2 * t][0], z[2 * t][1], z[2 * t + 1][0], z[2 * t + 1][1]};
        xpr = mfma_h(FR[t], zp, xpr);
        xpi = mfma_h(FI[t], zp, xpi);
      }
#pragma unroll
      for (int t = 0; t < 4; ++t) {
        const u32x4 zm = {z[2 * t][2], z[2 * t][3], z[2 * t + 1][2], z[2 * t + 1][3]};
        xmr = mfma_h(FR[t], zm, xmr);
        xmi = mfma_h(FI[t], zm, xmi);
      }
      f32x16 ypr = {}, ypi = {}, ymr = {}, ymi = {};
#pragma unroll
      for (int t = 0; t < 4; ++t) {
        u32x4 pp;
#pragma unroll
        for (int e = 0; e < 2; ++e) {
          const int r = 4 * t + 2 * e, q = 2 * t + e;
          pp[e] = pk_bf2(fma2(pr(xpr, r), wpr[q], -(pr(xpi, r) * wpi[q])));
          pp[2 + e] = pk_bf2(fma2(pr(xpr, r), wpi[q], pr(xpi, r) * wpr[q]));
        }
        ypr = mfma_bf(pp, IR[t], ypr);
        ypi = mfma_bf(pp, II[t], ypi);
      }
#pragma unroll
      for (int t = 0; t < 4; ++t) {
        u32x4 pm;
#pragma unroll
        for (int e = 0; e < 2; ++e) {
          const int r = 4 * t + 2 * e, q = 2 * t + e;
          pm[e] = pk_bf2(fma2(pr(xmr, r), wmr[q], -(pr(xmi, r) * wmi[q])));
          pm[2 + e] = pk_bf2(fma2(pr(xmr, r), wmi[q], pr(xmi, r) * wmr[q]));
        }
        ymr = mfma_bf(pm, IR[t], ymr);
        ymi = mfma_bf(pm, II[t], ymi);
      }
#pragma unroll
      for (int q = 0; q < 4; ++q) {
        u32x4 uc, us;
#pragma unroll
        for (int e = 0; e < 2; ++e) {
          const int r = 4 * q + 2 * e;
          f32x2 acr, aci, asr, asi;
          if (q == 0) {
            acr = fma2(bc(ff), pr(ymr, r), pr(ypr, r));
            aci = fma2(bc(ff), pr(ymi, r), pr(ypi, r));
            asr = fma2(bc(cph), pr(ymi, r), fma2(bc(sph), pr(ymr, r), -(bc(ff) * pr(ypi, r))));
            asi = fma2(bc(sph), pr(ymi, r), fma2(bc(ff), pr(ypr, r), -(bc(cph) * pr(ymr, r))));
          } else {
            acr = pr(ypr, r) + pr(ymr, r);
            aci = pr(ypi, r) + pr(ymi, r);
            asr = pr(ymi, r) - pr(ypi, r);
            asi = pr(ypr, r) - pr(ymr, r);
          }
          uc[e] = pk_bf2(acr); uc[2 + e] = pk_bf2(aci);
          us[e] = pk_bf2(asr); us[2 + e] = pk_bf2(asi);
        }
        lds128w(tw + (unsigned)(((8 * h + 2 * q) ^ (l31 & 7)) * 16), uc);
        lds128w(tw + (unsigned)(((8 * h + 2 * q + 1) ^ (l31 & 7)) * 16), us);
      }
#pragma unroll
      for (int i = 0; i < 8; ++i) {
        const u32x4 v = lds128(tr + (unsigned)(i * 1024 + (((lane & 15) ^ ((4 * i + (lane >> 4)) & 7)) * 16)));
        __builtin_amdgcn_raw_buffer_store_b128(v, rws, ao + i * 8 * 29 * 1024, 0, 16);
      }
    }
    WAITBAR(0);
    if (w == 0) flag_store(rws, WS_AF + (unsigned)((pq * 4 + qt) * 128), lane, tag);
    return;
  }

  auto prog = [&](unsigned v) INL {
    asm volatile("" ::: "memory");
    *(volatile LAS unsigned*)(LB + L_PROG + w * 4) = v;
    asm volatile("" ::: "memory");
  };
  if (w >= 4) {
    *(volatile LAS unsigned*)(LB + L_PROG + (lane & 7) * 4) = 0;
    asm volatile("s_waitcnt lgkmcnt(0)" ::: "memory");
  }
  asm volatile("s_barrier" ::: "memory");
  const int q4 = (loc * 37) >> 8, j7 = loc - 7 * q4;
  const int rg = 4 * (xcd >> 1) + q4, j = 7 * (xcd & 1) + j7;
  const int rgw = w & 3, grp = w >> 2;
  const int par = rgw & 1, tb = rgw >> 1;
  const unsigned rot = __builtin_elementwise_min((unsigned)(j7 + 2 * q4), (unsigned)(j7 + 2 * q4 - 7));
  const int s2 = 2 * rg + (xcd & 1), cg = j7;
  u32x4 sw1, sw2, sws;
  rsrc_t rws;
  auto args = [&]() INL {
    sw1 = mk_srd(argp(ka, 1), 448 * 1792 * 2);
    sw2 = mk_srd(argp(ka, 3), 448 * 1792 * 2);
    sws = mk_srd(argp(ka, 2), WS_END);
    rws = __builtin_amdgcn_make_buffer_rsrc(argp(ka, 2), 0, WS_END, 0x00020000);
  };
  const unsigned swz16 = (unsigned)(((lane & 7) ^ ((lane >> 2) & 4)) * 16);
  const unsigned vw1 = (unsigned)((lane >> 3) * 3584 + 128 * j + 1792 * grp) + swz16;
  const unsigned vw2 = (unsigned)(((lane >> 3) + 224 * rgw) * 896 + 128 * cg) + swz16, vw2b = vw2 + 6144u;
  const unsigned bE = LB + L_E + rgw * 4096, bY = LB + rgw * 2048;
  const unsigned b5O = bY + L_C0 + (rgw < 2 ? 53248u : -4096u), b5Y = rgw == 2 ? LB + L_E : b5O - L_C0;
  const unsigned pa = LB + L_PROG + (lane & 7) * 4;
#define W2O(t) ((56 * ((t) / 14) + (t) % 14) * 14336)
#define SLOT_R(t) ((t) < 16 || (t) > 24 ? (t) : (t) - 16)
#define SLOT_I(t) (SLOT_R(t) < 12 ? SLOT_R(t) - 5 : SLOT_R(t) < 16 ? SLOT_R(t) - 12 : SLOT_R(t) - 21)
#define SLOT_O(t) (SLOT_R(t) == 0 ? 2048 : SLOT_R(t) < 3 ? 14336 + 2048 * SLOT_R(t) : SLOT_R(t) < 5 ? 26624 + 2048 * SLOT_R(t) : \
                   SLOT_I(t) < 5 ? 12288 + 8192 * SLOT_I(t) : SLOT_I(t) == 5 ? 0 : 4096)
  const unsigned bO = bY + L_C0;
  auto slot_b = [&](int t) INL { const int r = SLOT_R(t); return r < 5 ? bE : r == 10 ? b5O : r == 26 ? b5Y : r < 12 ? bO : bY; };
  auto w2dma = [&](auto tc) INL { constexpr int t = tc; dma_w2<1>(slot_b(t), SLOT_O(t), vw2, vw2b, sw2, W2O(t)); };
  const unsigned wr0 = 12u + (unsigned)rgw + 8u * rot;
  unsigned wrow = __builtin_elementwise_min(wr0, wr0 - 56u) * 28672u;
  const unsigned lw = LB + (grp ? 0u : L_C0) + rgw * 1024;
  auto wdma = [&](auto rc) INL {
    constexpr int r = rc;
    if constexpr (r == 11) dma_w1((grp && rgw < 2) ? lw + L_E : lw, 0, vw1, sw1, wrow);
    else dma_w1(lw, ((12 + 4 * r) % 56) * 1024, vw1, sw1, wrow);
  };

  const int tq = (lane >> 2) & 3, tp = lane & 3, tg = (lane >> 4) & 1;
  unsigned w1a[8], oo, csa, b1a;
  u32x2 one;
  auto g1setup = [&]() INL {
    const unsigned w1l = (unsigned)((8 * h + tq) * 128 + 64 * ((tq >> 1) ^ tb) + 32 * tg + 8 * tp);
    const unsigned b = LB + (grp ? 0u : L_C0), k0 = LB + (grp ? L_E : L_C0);
    w1a[0] = b + 2048u * par + w1l;
    w1a[2] = b + 2048u * (1 - par) + w1l;
    w1a[4] = (par ? b + 2048u : k0) + w1l;
    w1a[6] = (par ? k0 : b + 2048u) + w1l;
#pragma unroll
    for (int i = 0; i < 8; i += 2) { w1a[i + 1] = w1a[i] ^ 64u; PIN(w1a[i]); PIN(w1a[i + 1]); }
    one = u32x2{0x3f803f80u, 0x3f803f80u};
    oo = (unsigned)((32 * (tid >> 3) + s2) * 896 + 128 * cg + (tid & 7) * 16);
    csa = LB + L_CS + (unsigned)(grp * 4096 + tb * 512 + lane * 8);
    b1a = LB + L_B1 + (unsigned)(grp * 128 + tb * 64 + l31 * 2);
    PIN(one); PIN(oo); PIN(csa); PIN(b1a);
  };
  u32x4 X[28];
  unsigned xa4[2][4];
  auto xaddr = [&]() INL {
#pragma unroll
    for (int u = 0; u < 8; ++u) {
      xa4[u >> 2][u & 3] = LB + (unsigned)((32 * rgw + l31) * 128 + (((h ^ ((l31 >> 1) & 7)) ^ (2 * ((u & 3) ^ par))) * 16)) + xblk(u & 4);
      PIN(xa4[u >> 2][u & 3]);
    }
  };
  auto xread = [&](int u) INL { X[u] = lds128(xa4[u >> 4][u & 3] + 16384u * ((u >> 2) & 3)); };
  u32x2 tag;
  u32x4 xres;
  unsigned ms;
  float rstd;

  auto xload = [&]() INL {
    xres = __builtin_amdgcn_raw_buffer_load_b128(__builtin_amdgcn_make_buffer_rsrc(argp(ka, 0), 0, 2048 * 448 * 2, 0x00020000), oo, 0, 0);
  };
  if (__builtin_expect(grp == 0, 1)) {
    unsigned vx0 = (unsigned)((32 * (32 * (rgw & 1) + (lane >> 3)) + 2 * rg + (rgw >> 1)) * 896 + (((lane & 7) ^ (lane >> 4)) * 16));
    unsigned vx1 = (vx0 ^ 64u) + 228352u, vx2 = vx0 + 2 * 228352u, vx3 = vx1 + 2 * 228352u;
    unsigned xcol = rot * 128u, lx = LB + rgw * 4096;
    PIN(vx0); PIN(vx1); PIN(vx2); PIN(vx3);
    asm volatile("s_setprio 3" : "+s"(xcol), "+s"(lx) :: "memory");
    const u32x4 sx = mk_srd((const void*)landed(), 2048 * 448 * 2);
    auto xdma = [&](auto kc) INL { dma_x(lx, xblk(kc), vx0, vx1, vx2, vx3, sx, xcol); };
    sfor<0, 3>(xdma);
    ARGS_LOAD();
    TAG_LOAD();
    SB();
    touch(LB + L_B1, (unsigned)(128 * j + 1792 * h + 4 * l31), mk_srd(argp(ka, 4), 1792 * 2));
    touch(LB + L_B2, (unsigned)(128 * cg + 4 * lane), mk_srd(argp(ka, 5), 448 * 2));
    args();
    xaddr();
    unsigned vt2 = lane < 14 ? (unsigned)((56 * (4 * xcd + q4) + 14 * rgw + lane) * 896 + 128 * cg) : OOB;
    PIN(vt2);
    const unsigned tr0 = 96u + 64u * rot + (unsigned)(32 * rgw + l31);
    unsigned vt = h ? OOB : __builtin_elementwise_min(tr0, tr0 - 448u) * 3584u + (unsigned)(128 * j + 1792);
    PIN(vt);
    g1setup();
    unsigned sta = LB + (unsigned)(32 * rgw + l31) * 4u;
    PIN(sta);
    f32x4 sa = {}, sb = {}, qa = {}, qb = {};
    sfor<0, 7>([&](auto ic) INL {
      constexpr int i = ic;
      asm volatile("s_waitcnt vmcnt(%0)" :: "n"(i < 3 ? 10 : 8) : "memory");
      prog(i + 1);
#pragma unroll
      for (int u = 4 * i; u < 4 * i + 4; ++u) xread(u);
      SB();
      if constexpr (i < 4) xdma(std::integral_constant<int, i + 3>{});
      if constexpr (i == 4) sfor<0, 4>(wdma);
      if constexpr (i == 5) {
        sfor<4, 7>(wdma);
        touch(LB + 256, vt, sw1);
      }
      if constexpr (i == 6) sfor<7, 11>(wdma);
      SB();
#pragma unroll
      for (int u = 4 * i; u < 4 * i + 4; ++u) {
        sa = mfma4(one[0], one[1], X[u][0], X[u][1], sa);
        qa = mfma4(X[u][0], X[u][1], X[u][0], X[u][1], qa);
        sb = mfma4(one[0], one[1], X[u][2], X[u][3], sb);
        qb = mfma4(X[u][2], X[u][3], X[u][2], X[u][3], qb);
      }
      SB();
    });
    qa += qb;
    const float q01 = (lane & 1) ? qa[1] : qa[0], q23 = (lane & 1) ? qa[3] : qa[2];
    const float mean = half_sum(sa[0] + sb[0]) * (1.0f / 448.0f);
    const float var = __builtin_fmaxf(__builtin_fmaf(-mean, mean, half_sum((lane & 2) ? q23 : q01) * (1.0f / 448.0f)), 0.0f);
    ms = pk_bf(-mean, __builtin_amdgcn_sqrtf(bf16r(bf16r(var) + 1e-5f)));
    rstd = __builtin_amdgcn_rcpf(bf_hi(ms));
    *(LAS unsigned*)(sta + L_XA) = ms;
    *(LAS float*)(sta + L_RS) = rstd;
    prog(8);
    __builtin_amdgcn_s_setprio(0);
    WAITVM(5);
    prog(9);
    wait_prog(pa, 5, 0xff);
    sfor<11, 14>(wdma);
    touch(LB + 256, vt2, sw2);
    tag = get_tag();
    wait_prog(pa, 9, 0x0f);
    __builtin_amdgcn_s_setprio(2);
  } else {
    u32x4 F[28];
    {
      const bool sp = (j == 0) && (l31 == 16);
      const float kdf = sp ? 224.0f : (float)(16 * j + (l31 & 15));
      const float ph = ((l31 >> 4) && !sp) ? 0.25f : 0.0f;
      const float tw = 2.0f * cosr(rev448(32.0f * kdf));
      const float d0 = (float)(8 * h + 64 * (int)rot), dl = kdf * (1.0f / 448.0f);
      float f[4][8];
#pragma unroll
      for (int i = 0; i < 28; ++i) {
        if (i < 4) {
          const float a = rev448((d0 + (float)(16 * (i ^ par))) * kdf) + ph;
#pragma unroll
          for (int e = 0; e < 8; ++e) f[i][e] = cosr(__builtin_fmaf((float)e, dl, a));
        } else {
#pragma unroll
          for (int e = 0; e < 8; ++e) f[i & 3][e] = __builtin_fmaf(tw, f[(i + 2) & 3][e], -f[i & 3][e]);
        }
#pragma unroll
        for (int e = 0; e < 4; ++e) F[i][e] = pk_bf(f[i & 3][2 * e], f[i & 3][2 * e + 1]);
        PIN(F[i]);
      }
    }
    ARGS_LOAD();
    TAG_LOAD();
    args();
    u32x4 GA[2][2];
#pragma unroll
    for (int mt = 0; mt < 2; ++mt) {
      const int hh = (l31 >> 2) & 1, reg = (l31 & 3) + 4 * (l31 >> 3);
      const int s2a = 2 * rg + (rgw >> 1);
      const int pp = 16 * mt + 8 * hh + (reg >> 1), cs = reg & 1;
      const bool sp = (pp == 0) && cs;
      const float ca = sp ? 0.5f : (float)pp * (1.0f / 64.0f);
      float cb = (float)(pp * s2a) * (1.0f / 2048.0f) - 0.25f * (float)cs;
      PIN(cb);
      cb = __builtin_fmaf(ca, (float)(32 * (rgw & 1) + 4 * h), sp ? 0.0f : cb);
      const float tw = 2.0f * cosr(8.0f * ca);
      float g[4][4];
#pragma unroll
      for (int e = 0; e < 4; ++e) {
        g[0][e] = cosr(__builtin_fmaf(ca, (float)e, cb));
        g[1][e] = cosr(__builtin_fmaf(ca, (float)(e + 8), cb));
        g[2][e] = __builtin_fmaf(tw, g[1][e], -g[0][e]);
        g[3][e] = __builtin_fmaf(tw, g[2][e], -g[1][e]);
      }
#pragma unroll
      for (int e = 0; e < 8; ++e) GA[mt][e >> 2][e & 3] = pk_h(g[e >> 1][2 * (e & 1)], g[e >> 1][2 * (e & 1) + 1]);
      PIN(GA[mt][0]); PIN(GA[mt][1]);
    }
    const int p = (tid & 255) >> 3, kdl = tid & 7;
    const float ff = p ? 1.0f : 0.0f;
    f16x2 k1 = h2(1.0f, -ff), k2 = h2(ff, 1.0f), k3[2], k4[2], k5[2];
#pragma unroll
    for (int sg = 0; sg < 2; ++sg) {
      const float ang = p ? 0.0f : (float)(2 * rg + sg) * (1.0f / 64.0f);
      const float c = cosr(ang), sn = sinr(ang);
      k3[sg] = h2(ff, c); k4[sg] = h2(-c, ff); k5[sg] = h2(sn, 0.0f);
      PIN(k3[sg]); PIN(k4[sg]); PIN(k5[sg]);
    }
    PIN(k1); PIN(k2);
    xaddr();
    unsigned sta, rra, gw[4], ga, spm, zo, zo1, zxo;
    u32x4 faug, xu;
    tag = get_tag();
    f32x16 U = {};
    unsigned have = 0, po = LB + L_PROG + rgw * 4;
    PIN(po);
    sfor<0, 8>([&](auto ic) INL {
      constexpr int i = ic;
      if constexpr (i < 7) {
        wait_one<i + 1>(po, have);
#pragma unroll
        for (int u = 4 * i; u < 4 * i + 4; ++u) xread(u);
        SB();
      }
      if constexpr (i > 0) {
#pragma unroll
        for (int u = 4 * i - 4; u < 4 * i; ++u) U = mfma_bf(X[u], F[u], U);
        SB();
      }
      if constexpr (i == 3) {
        sta = LB + (unsigned)(32 * rgw + l31) * 4u, rra = LB + L_RS + (unsigned)(rgw * 32 + 4 * h) * 4u;
        ga = LB + L_GATH + (unsigned)(kdl * 128 + (((p >> 2) ^ kdl) * 16) + (p & 3) * 4);
#pragma unroll
        for (int q = 0; q < 4; ++q) {
          gw[q] = LB + L_GATH + (unsigned)(rgw * 4096 + l31 * 128 + (((4 * (q >> 1) + 2 * h + (q & 1)) ^ (l31 & 7)) * 16));
          PIN(gw[q]);
        }
        PIN(sta); PIN(rra); PIN(ga);
      }
      if constexpr (i == 4) {
        const bool sp = (j == 0) && (kdl == 0);
        spm = sp ? 0u : ~0u;
        zo = WS_Z + (unsigned)((2 * j * 16 + rg) * 4096 + (tid & 255) * 16), zo1 = zo + 65536u;
        zxo = sp ? WS_Z + (unsigned)((28 * 16 + rg) * 4096 + p * 128) : OOB;
        faug = u32x4{(j == 0 && lane == 0) ? 0x43e0u : 0u, 0u, 0u, 0u};
        xu = u32x4{0u, 0u, 0u, 0u};
        PIN(spm); PIN(zo); PIN(zo1); PIN(zxo); PIN(faug); PIN(xu);
      }
      if constexpr (i == 5) prog(5);
      SB();
    });
    PIN(U);
    __builtin_amdgcn_s_setprio(3);
    wait_one<8>(po, have);
    f32x4 rr[4];
    xu[0] = *(LAS unsigned*)(sta + L_XA);
#pragma unroll
    for (int q = 0; q < 4; ++q) rr[q] = *(LAS f32x4*)(rra + 32 * q);
    rstd = *(LAS float*)(sta + L_RS);
    SB();
    ms = xu[0];
    U = mfma_bf(xu, faug, U);
    u32x4 ub[2];
#pragma unroll
    for (int q = 0; q < 4; ++q) {
      ub[q >> 1][2 * (q & 1)] = pk_h(U[4 * q] * rr[q][0], U[4 * q + 1] * rr[q][1]);
      ub[q >> 1][2 * (q & 1) + 1] = pk_h(U[4 * q + 2] * rr[q][2], U[4 * q + 3] * rr[q][3]);
    }
    f32x16 za[2] = {};
#pragma unroll
    for (int t = 0; t < 2; ++t)
#pragma unroll
      for (int mt = 0; mt < 2; ++mt) za[mt] = mfma_h(GA[mt][t], ub[t], za[mt]);
#pragma unroll
    for (int q = 0; q < 4; ++q) {
      u32x4 v;
#pragma unroll
      for (int e = 0; e < 4; ++e) v[e] = pk_h2(pr(za[q >> 1], 8 * (q & 1) + 2 * e));
      lds128w(gw[q], v);
    }
    prog(6);
    wait_prog(pa, 5, 0xff);
    sfor<0, 7>(wdma);
    wait_prog(pa, 6, 0xf0);
    f16x2 gv[16];
#pragma unroll
    for (int n = 0; n < 16; ++n) gv[n] = *(LAS f16x2*)(ga + (unsigned)((n >> 3) * 1024 + ((n >> 2) & 1) * 8192 + ((n >> 1) & 1) * 2048 + (n & 1) * 4096));
    SB();
    u32x4 zx;
#pragma unroll
    for (int gl = 0; gl < 2; ++gl) {
      u32x4 z;
#pragma unroll
      for (int sg = 0; sg < 2; ++sg) {
        const f16x2 t0 = gv[8 * gl + 4 * sg] + gv[8 * gl + 4 * sg + 1];
        f16x2 t1 = gv[8 * gl + 4 * sg + 2] + gv[8 * gl + 4 * sg + 3];
        if (gl == 0) {
          zx[sg] = __builtin_bit_cast(unsigned, t1);
          t1 = __builtin_bit_cast(f16x2, zx[sg] & spm);
        }
        z[sg] = __builtin_bit_cast(unsigned, hfma(t0, k1, t1.yx * k2));
        z[2 + sg] = __builtin_bit_cast(unsigned, hfma(t1.yy, k5[sg].yx, hfma(t0.yy, k5[sg], hfma(t0, k3[sg], t1.yx * k4[sg]))));
      }
      __builtin_amdgcn_raw_buffer_store_b128(z, rws, gl ? zo1 : zo, 0, 16);
      SB();
    }
#pragma unroll
    for (int sg = 0; sg < 2; ++sg) {
      const f16x2 t1 = __builtin_bit_cast(f16x2, (unsigned)zx[sg]);
      zx[sg] = __builtin_bit_cast(unsigned, t1 * k1);
      zx[2 + sg] = __builtin_bit_cast(unsigned, hfma(t1.yy, k5[sg], t1 * k3[sg]));
    }
    __builtin_amdgcn_raw_buffer_store_b128(zx, rws, zxo, 0, 16);
    SB();
    __builtin_amdgcn_s_setprio(1);
    sfor<7, 14>(wdma);
    g1setup();
    WAITVM(10);
    prog(9);
    sfor<0, 3>(w2dma);
    xload();
    WAITVM(14);
    flag_store(rws, WS_ZF + (unsigned)((rg * 14 + j) * 512 + rgw * 128), lane, tag);
    wait_prog(pa, 9, 0xf0);
    wait_prog(pa, 12, 0x0f);
  }
  const unsigned gmask = grp ? 0xf0u : 0x0fu;

#define SEQ_U(i) (((i) + 6) % 28)
  auto frag2 = [&](unsigned a) INL { return trfrag(a, a + 512); };
  f32x16 h0 = {}, h1 = {};
  {
    u32x4 wf[28][2];
    auto wload = [&](int u, int x) INL { wf[u][x] = frag2(w1a[(SEQ_U(u) < 2 ? 4 : 0) + 2 * (u & 1) + x] + 2048u * (SEQ_U(u) & ~1)); };
#pragma unroll
    for (int u = 0; u < 6; ++u) { wload(u >> 1, u & 1); SB(); }
    u32x4 xaug = {__builtin_amdgcn_perm(ms, ms, 0x01000100u), h ? ms >> 16 : 0u, 0u, 0u}, a0 = {}, a1 = {};
    PIN(xaug); PIN(a0); PIN(a1);
    f32x4 ca = {}, cb = {}, cc = {}, cd = {};
    f32x2 e0, e1;
    unsigned pv, late, late2;
    sfor<0, 28>([&](auto uc) INL {
      constexpr int u = uc, c = (u & 3) == 2 && u < 20 ? u : u == 16 ? 22 : u == 20 ? 26 : -1;
      constexpr bool chk = u == 10 || u == 25;
      h0 = mfma_bf(wf[u][0], X[SEQ_U(u)], h0);
      if constexpr (u + 3 < 20 || (u + 3 < 28 && !(u & 1))) wload(u + 3, 0);
      if constexpr (u >= 11 && u <= 17 && (u & 1)) wload(u + 9, 0);
      if constexpr (u == 10) wload(u + 3, 1);
      if constexpr (u == 8) asm volatile("s_waitcnt vmcnt(14)\n\ts_cmp_lg_u32 %0, 0\n\ts_cbranch_scc1 1f\n\ts_waitcnt vmcnt(1)\n1:" :: "s"(grp) : "memory", "scc");
      if constexpr (u == 8 || u == 14 || u == 25) prog(u == 8 ? 11 : u == 14 ? 12 : 14);
      if constexpr (u == 9 || u == 24) pv = *(volatile LAS unsigned*)pa;
      if constexpr (chk) {
        late = (unsigned)__builtin_amdgcn_ballot_w64(pv < (u == 10 ? 10u : 13u));
        asm volatile("" : "+s"(late));
      }
      if constexpr (u == 10) {
        late2 = (unsigned)__builtin_amdgcn_ballot_w64(pv < 14u);
        asm volatile("" : "+s"(late2));
      }
      if constexpr (u == 26) {
        e0 = *(LAS f32x2*)csa;
        e1 = *(LAS f32x2*)(csa ^ 512u);
      }
      if constexpr (u == 27) {
        const float s0 = e0[0] + e0[1], t0 = bf16r(s0);
        a0[0] = pk_bf(t0, s0 - t0);
      }
      SB();
      h1 = mfma_bf(wf[u][1], X[SEQ_U(u)], h1);
      if constexpr (u + 3 < 28 && u != 10) wload(u + 3, 1);
      if constexpr (u == 27) {
        const float s1 = e1[0] + e1[1], t1 = bf16r(s1);
        a1[0] = pk_bf(t1, s1 - t1);
      }
      if constexpr (u == 21) {
        *(LAS float*)(csa + (unsigned)(par * 4)) = (ca[0] + cb[0]) + (cc[0] + cd[0]);
        WAITVM(6);
        prog(13);
      }
      if constexpr (u == 23) {
        a0[1] = *(LAS unsigned short*)b1a;
        a1[1] = *(LAS unsigned short*)(b1a ^ 64u);
      }
      SB();
      if constexpr (chk) { check_prog<(u == 10 ? 10 : 13)>(late, pa, gmask); SB(); }
      if constexpr (u == 14) { check_prog<14>(late2, pa, gmask >> 4); SB(); }
      if constexpr (u >= 3 && u <= 19 && (u & 1)) {
        constexpr int t = (u + 3) / 2;
        dma_w2_if(grp, slot_b(t), SLOT_O(t), vw2, vw2b, sw2, W2O(t));
        SB();
      }
      if constexpr (c >= 0) {
        ca = mfma4(one[0], one[1], wf[c - 2][0][0], wf[c - 2][0][1], ca);
        cb = mfma4(one[0], one[1], wf[c - 2][0][2], wf[c - 2][0][3], cb);
        cc = mfma4(one[0], one[1], wf[c][0][0], wf[c][0][1], cc);
        cd = mfma4(one[0], one[1], wf[c][0][2], wf[c][0][3], cd);
        SB();
      }
    });
    h0 = mfma_bf(a0, xaug, h0);
    h1 = mfma_bf(a1, xaug, h1);
    PIN(h0); PIN(h1);
  }
  const float sig = bf_hi(ms), gc1 = -2.302208198f * rstd, gc2 = gc1 * 0.044715f * rstd * rstd;
  const unsigned ho = WS_H + (unsigned)((((2 * rg + (rgw >> 1)) * 112 + 56 * grp + 4 * j + 2 * tb) * 2 + (rgw & 1)) * 1024 + lane * 16);
  u32x4 hq[4];
  auto gelu0 = [&](auto tc) INL {
    constexpr int t = tc;
    f32x16& hv = t < 2 ? h0 : h1;
    PIN(hv);
    float g[8];
#define G8(expr) _Pragma("unroll") for (int e = 0; e < 8; ++e) { const float u = hv[8 * (t & 1) + e]; (void)u; g[e] = (expr); } SB();
    G8(u * u)
    G8(__builtin_fmaf(g[e], gc2, gc1))
    G8(g[e] * u)
    G8(__builtin_amdgcn_exp2f(g[e]))
    G8(__builtin_fmaf(g[e], sig, sig))
    G8(__builtin_amdgcn_rcpf(g[e]))
    G8(g[e] * u)
    hq[t] = u32x4{pk_bf(g[0], g[1]), pk_bf(g[2], g[3]), pk_bf(g[4], g[5]), pk_bf(g[6], g[7])};
    SB();
  };
  auto hstore = [&](auto tc) INL {
    constexpr int t = tc;
    __builtin_amdgcn_raw_buffer_store_b128(hq[t], rws, (t < 2 ? ho : ho ^ 4096u) + (t & 1) * 2048, 0, 16);
    SB();
  };
  auto gelu = [&](auto tc) INL { gelu0(tc); hstore(tc); };

  const rsrc_t rout = __builtin_amdgcn_make_buffer_rsrc(argp(ka, 6), 0, 2048 * 448 * 2, 0x00020000);
  const unsigned hfo = WS_HF + (unsigned)((rg * 14 + j) * 1024 + w * 128);
  const int rho = rgw & 1, kap = rgw >> 1;
  const unsigned tfa = bY + (unsigned)(12288 + lane * 16);
  float r[8];
  unsigned ra;
  auto rinit = [&]() INL {
    ra = LB + L_C0 + (unsigned)(12288 + (tid >> 7) * 8192 + ((tid >> 3) & 15) * 128 + (((tid & 7) ^ ((tid >> 3) & 7)) * 16));
    PIN(ra);
    const u32x4 b2v = lds128(LB + L_B2 + (unsigned)((tid & 7) * 16));
#pragma unroll
    for (int e = 0; e < 4; ++e) {
      r[2 * e] = bf_lo(xres[e]) + bf_lo(b2v[e]);
      r[2 * e + 1] = bf_hi(xres[e]) + bf_hi(b2v[e]);
      PIN(r[2 * e]); PIN(r[2 * e + 1]);
    }
  };
  if (grp) {
    sfor<0, 1>(gelu);
    WAITVM(1);
    prog(15);
    sfor<1, 4>(gelu);
    __builtin_amdgcn_s_setprio(0);
    u32x4 I[15][2];
#pragma unroll
    for (int x = 0; x < 2; ++x) {
      const float d = (float)(64 * cg + 32 * x + l31), dl = d * (1.0f / 448.0f);
      const float tw = 2.0f * cosr(rev448(8.0f * d));
      const float k0 = (float)(112 * kap + 4 * h);
      const float a0 = rev448(k0 * d), a1 = rev448((k0 + 8.0f) * d);
      float f0[8], f1[8];
#pragma unroll
      for (int e = 0; e < 4; ++e) {
        const float b0 = __builtin_fmaf((float)e, dl, a0), b1 = __builtin_fmaf((float)e, dl, a1);
        f0[e] = cosr(b0); f0[4 + e] = sinr(-b0);
        f1[e] = cosr(b1); f1[4 + e] = sinr(-b1);
      }
#pragma unroll
      for (int i = 0; i < 15; ++i) {
#pragma unroll
        for (int e = 0; e < 4; ++e) I[i][x][e] = pk_bf(f0[2 * e], f0[2 * e + 1]);
#pragma unroll
        for (int e = 0; e < 8; ++e) {
          const float n = __builtin_fmaf(tw, f1[e], -f0[e]);
          f0[e] = f1[e];
          f1[e] = n;
        }
        PIN(I[i][x]);
      }
      if (x == 0) {
        WAITVM(0);
        flag_store(rws, hfo, lane, tag);
      }
    }
    u32x4 GC[2][2];
#pragma unroll
    for (int sh = 0; sh < 2; ++sh) {
      const float sf = (float)(32 * (32 * sh + l31) + s2) * (1.0f / 2048.0f);
#pragma unroll
      for (int t = 0; t < 2; ++t)
#pragma unroll
        for (int e = 0; e < 4; ++e) {
          const int p = 16 * rho + 8 * t + 4 * (e >> 1) + 2 * h + (e & 1);
          const float a = (float)p * sf;
          float sv = sinr(a);
          if (t == 0 && e == 0) sv = (p == 0) ? ((l31 & 1) ? -1.0f : 1.0f) : sv;
          GC[sh][t][e] = pk_bf(cosr(a), sv);
        }
      PIN(GC[sh][0]); PIN(GC[sh][1]);
    }
    unsigned ai = WS_A + (unsigned)(((s2 * 2 + rho) * 29 + 14 * kap) * 1024 + (l31 >> 3) * 256 + h * 128 + (l31 & 7) * 16);
    PIN(ai);
    rinit();
    wait_prog(pa, 15, 1u << rgw);
    __builtin_amdgcn_s_setprio(3);
    poll(sws, WS_AF + (unsigned)(((4 * rho + (lane & 3)) * 4 + 2 * kap + ((lane >> 2) & 1)) * 128), tag);
    u32x4 A[15];
#pragma unroll
    for (int i = 0; i < 15; ++i) A[i] = __builtin_amdgcn_raw_buffer_load_b128(rws, (i == 14 && kap == 0) ? OOB : ai, i * 1024, 16);
    SB();
    f32x16 T[2] = {};
#pragma unroll
    for (int i = 0; i < 15; ++i) {
      T[0] = mfma_bf(A[i], I[i][0], T[0]);
      T[1] = mfma_bf(A[i], I[i][1], T[1]);
      if (i == 8) {
        SB();
        wait_prog(pa, 16, 1u << rgw);
#pragma unroll
        for (int n = 0; n < 4; ++n) lds128w(tfa + (unsigned)(16384 + (n >> 1) * 8192 + (n & 1) * 1024), GC[n >> 1][n & 1]);
        SB();
      }
    }
#pragma unroll
    for (int x = 0; x < 2; ++x)
#pragma unroll
      for (int t = 0; t < 2; ++t) {
        u32x4 tf;
#pragma unroll
        for (int e = 0; e < 4; ++e) tf[e] = pk_bf2(pr(T[x], 8 * t + 2 * e));
        lds128w(tfa + (unsigned)(x * 8192 + t * 1024), tf);
      }
    prog(16);
    __builtin_amdgcn_s_setprio(0);
    asm volatile("" : "=v"(h0), "=v"(h1));
  }
  int grp2 = grp;
  asm volatile("" : "+s"(grp2));
  if (grp2 == 0) {
    f32x16 acc[2][2] = {};
    const unsigned hpo = WS_HF + (unsigned)((rg * 14 + ((14 * rgw) >> 2) + ((lane >> 1) & 3)) * 1024 + (2 * (s2 & 1) + (lane & 1)) * 128);
    const unsigned hi = WS_H + (unsigned)((s2 * 112 + 14 * rgw) * 2048 + lane * 16);
    const unsigned w2l = (unsigned)((4 * h + tq) * 128 + 64 * (tq >> 1) + 32 * tg + 8 * tp);
    u32x4 H[28][2], W[28][2];
    auto hload = [&](int t) INL {
#pragma unroll
      for (int sh = 0; sh < 2; ++sh) H[t][sh] = __builtin_amdgcn_raw_buffer_load_b128(rws, hi + sh * 1024, (56 * (t / 14) + t % 14) * 2048, 16);
    };
    auto wload = [&](int t) INL {
#pragma unroll
      for (int x = 0; x < 2; ++x) {
        const unsigned a = slot_b(t) + (w2l ^ (64u * x)) + SLOT_O(t);
        W[t][x] = trfrag(a, a + 1024);
      }
    };
#define FLAG_LOAD(v, o) asm volatile("buffer_load_dwordx2 %0, %1, %2, 0 offen offset:" #o " sc1" : "=v"(v) : "v"(hpo), "s"(sws) : "memory")
#define FLAG_BAD(v) (__builtin_amdgcn_ballot_w64(v[0] != tag[0] || v[1] != tag[1]) != 0)
    auto w2p = [&](auto tc) INL { WAITVM(8); w2dma(tc); };
    sfor<0, 4>(gelu);
    __builtin_amdgcn_s_setprio(0);
    wait_prog(pa, 13, 0xf0);
    sfor<12, 16>(w2dma);
#pragma unroll
    for (int t = 0; t < 9; ++t) wload(t);
    unsigned fo = lane < 8 ? hfo + (unsigned)lane * 16u : OOB;
    u32x4 fv = {tag[0], tag[1], tag[0], tag[1]};
    PIN(fo); PIN(fv);
#pragma unroll
    for (int t = 0; t < 9; ++t) { PIN(W[t][0]); PIN(W[t][1]); }
    WAITVM(8);
    __builtin_amdgcn_raw_buffer_store_b128(fv, rws, fo, 0, 16);
    u32x2 f0, f1;
    sfor<16, 22>(w2p);
    wait_prog(pa, 15, 0xf0);
    sfor<22, 28 - F0N>(w2p);
    FLAG_LOAD(f0, 0);
    sfor<28 - F0N, 28>(w2p);
    xload();
    FLAG_LOAD(f1, 0);
    asm volatile("s_waitcnt vmcnt(%1)" : "+v"(f0) : "n"(2 * F0N + 2) : "memory");
    if (FLAG_BAD(f0)) {
      asm volatile("s_waitcnt vmcnt(0)" : "+v"(f1) :: "memory");
      if (FLAG_BAD(f1)) poll(sws, hpo, tag);
    }
#pragma unroll
    for (int t = 0; t < HD; ++t) hload(t);
    SB();
    __builtin_amdgcn_s_setprio(2);
    SB();
    u32x4 tf[8];
    unsigned pb[8];
    auto part = [&](int n) INL {
      u32x2 v[4];
#pragma unroll
      for (int q = 0; q < 4; ++q) v[q] = u32x2{pk_bf2(pr(acc[n >> 1][n & 1], 4 * q)), pk_bf2(pr(acc[n >> 1][n & 1], 4 * q + 2))};
      SB();
#pragma unroll
      for (int q = 0; q < 4; ++q) *(LAS u32x2*)(pb[4 * (n >> 1) + q] + (unsigned)((n & 1) * 16384)) = v[q];
      SB();
    };
    sfor<0, 28>([&](auto tc) INL {
      constexpr int t = tc;
      if constexpr (t + 2 < 28 && t + 2 >= 9) wload(t + 2);
      acc[0][0] = mfma_bf(W[t][0], H[t][0], acc[0][0]);
      acc[0][1] = mfma_bf(W[t][0], H[t][1], acc[0][1]);
      SB();
      if constexpr (t == 0) asm volatile("" :: "v"(f1));
      if constexpr (t == TFL) FLAG_LOAD(f0, 512);
      if constexpr (t == TCH) {
        asm volatile("s_waitcnt vmcnt(%1)" : "+v"(f0) : "n"(2 * (14 - TFL - HD)) : "memory");
        PIN(acc[0][0]); PIN(acc[0][1]); PIN(acc[1][0]); PIN(acc[1][1]);
        if (FLAG_BAD(f0)) poll(sws, hpo + 512u, tag);
#pragma unroll
        for (int n = 14; n < TCH + HD; ++n) hload(n);
      }
      if constexpr (t + HD < 28 && (t + HD < 14 || t >= TCH)) hload(t + HD);
      SB();
      acc[1][0] = mfma_bf(W[t][1], H[t][0], acc[1][0]);
      if constexpr (t == 27) { SB(); part(0); }
      acc[1][1] = mfma_bf(W[t][1], H[t][1], acc[1][1]);
      SB();
      if constexpr (t == TA) prog(15);
      if constexpr (t == 14) prog(16);
      if constexpr (t == 5) rinit();
      if constexpr (t == 20) {
#pragma unroll
        for (int q = 0; q < 8; ++q) {
          pb[q] = bO + (unsigned)(12288 + (l31 >> 4) * 8192 + (l31 & 15) * 128 + h * 8 + ((l31 & 7) ^ q) * 16);
          PIN(pb[q]);
        }
      }
      if constexpr (t == TYY) {
        wait_prog(pa, 16, 16u << rgw);
#pragma unroll
        for (int n = 0; n < 8; ++n) tf[n] = lds128(tfa + (unsigned)((n >> 1) * 8192 + (n & 1) * 1024));
      }
      if constexpr (t == TYY + 1) {
#pragma unroll
        for (int n = 0; n < 8; ++n) acc[n & 1][(n >> 1) & 1] = mfma_bf(tf[2 * (n & 1) + (n >> 2)], tf[4 + (n & 2) + (n >> 2)], acc[n & 1][(n >> 1) & 1]);
      }
      SB();
    });
#pragma unroll
    for (int n = 1; n < 4; ++n) part(n);
    __builtin_amdgcn_s_setprio(0);
  }
  {
    unsigned slo = 0x00003f80u, shi = 0x3f800000u;
    asm volatile("" : "+s"(slo), "+s"(shi));
    LBAR();
    u32x4 v[4];
#pragma unroll
    for (int k = 0; k < 4; ++k) v[k] = lds128(ra + k * 2048);
    SB();
#pragma unroll
    for (int k = 0; k < 4; ++k)
#pragma unroll
      for (int e = 0; e < 4; ++e) {
        r[2 * e] = dot2(slo, v[k][e], r[2 * e]);
        r[2 * e + 1] = dot2(shi, v[k][e], r[2 * e + 1]);
      }
    const u32x4 o = {pk_bf(r[0], r[1]), pk_bf(r[2], r[3]), pk_bf(r[4], r[5]), pk_bf(r[6], r[7])};
    __builtin_amdgcn_raw_buffer_store_b128(o, rout, oo, 0, 16);
  }
  if (blockIdx.x == 0 && tid == 0) __builtin_amdgcn_raw_buffer_store_b64(tag, rws, WS_E, 0, 16);
}

extern "C" void kernel_launch(void* const* d_in, const int* in_sizes, int n_in, void* d_out, int out_size,
                              void* d_ws, size_t ws_size, hipStream_t stream) {
  fused_kernel<<<256, 512, 0, stream>>>(d_in[0], d_in[4], d_ws, d_in[6], d_in[5], d_in[7], d_out, d_in[1], d_in[2]);
}
```
